```python
import torch

def _softmax(x, axis=-1):
    return torch.softmax(x, dim=axis)
def _gelu(x):
    return torch.nn.functional.gelu(x, approximate="tanh")
def _silu(x):
    return torch.nn.functional.silu(x)
def _sigmoid(x):
    return torch.sigmoid(x)
def _layernorm(x, axis=-1, eps=1e-5):
    m = x.mean(dim=axis, keepdim=True)
    v = x.var(dim=axis, keepdim=True, unbiased=False)
    return (x - m) / torch.sqrt(v + eps)
def _scatter_add(tgt, idx, src, axis=0):
    return tgt.clone().index_add_(axis, idx, src)
def _segment_sum(src, seg, n):
    out = torch.zeros((n,) + src.shape[1:], dtype=src.dtype)
    return out.index_add_(0, seg, src)
def _pad(x, pw):
    flat = tuple(v for pair in reversed(pw) for v in pair)
    return torch.nn.functional.pad(x, flat)

kernel_name = 'standalone__window_attn_swin__vision'

def setup_inputs():

    _w = (lambda a: (lambda t: t.to(torch.bfloat16) if t.is_floating_point() else t)(torch.as_tensor(a)))
    H = W = 224
    C = 192
    ws = 8
    nh = 4
    shift = ws // 2
    coords = torch.stack(torch.meshgrid(torch.arange(ws), torch.arange(ws), indexing='ij'), 0).reshape(2, -1)
    rel = coords[:, :, None] - coords[:, None, :]
    rel[0] += ws - 1
    rel[1] += ws - 1
    rel[0] *= 2 * ws - 1
    rp_idx = rel.sum(0).to(torch.int64)
    img_mask = torch.zeros((1, H, W, 1), dtype=float)
    cnt = 0
    for hs in (slice(0, H - ws), slice(H - ws, H - shift), slice(H - shift, H)):
        for vs in (slice(0, W - ws), slice(W - ws, W - shift), slice(W - shift, W)):
            img_mask[:, hs, vs, :] = cnt
            cnt += 1
    mw = img_mask.reshape(1, H // ws, ws, W // ws, ws, 1).permute(0, 1, 3, 2, 4, 5).reshape(-1, ws * ws)
    am = mw[:, :, None] - mw[:, None, :]
    attn_mask = torch.where(am != 0, -100.0, 0.0).to(float)
    return {
        "x": _w((lambda *s: torch.randn(*s, dtype=torch.bfloat16) * 1.6)(1, H, W, C)),
        "wqkv": _w((lambda *s: torch.randn(*s, dtype=torch.bfloat16) * 1.6)(C, 3 * C) * 0.02),
        "wo": _w((lambda *s: torch.randn(*s, dtype=torch.bfloat16) * 0.05)(C, C) * 0.02),
        "rpb": _w((lambda *s: torch.randn(*s, dtype=torch.bfloat16) * 0.2)((2 * ws - 1) * (2 * ws - 1), nh)),
        "rp_idx": _w(rp_idx),
        "attn_mask": _w(attn_mask),
    }

def reference(x, wqkv, wo, rpb, rp_idx, attn_mask):
    B, H, W, C = x.shape
    N = rp_idx.shape[0]
    ws = int(round(N ** 0.5))
    nh = rpb.shape[-1]
    hd = C // nh
    shift = ws // 2
    xs = torch.roll(x, (-shift, -shift), (1, 2))
    nWh = H // ws
    nWw = W // ws
    xw = (lambda x, *d: x.permute(*d))(xs.reshape(B, nWh, ws, nWw, ws, C), 0, 1, 3, 2, 4, 5).reshape(B * nWh * nWw, N, C)
    qkv = (lambda x, *d: x.permute(*d))(torch.einsum('wnc,cd->wnd', xw, wqkv).reshape(-1, N, 3, nh, hd), 2, 0, 3, 1, 4)
    q, k, v = qkv[0], qkv[1], qkv[2]
    attn = torch.einsum('whid,whjd->whij', q, k) * (hd ** -0.5)
    bias = (lambda x, *d: x.permute(*d))(rpb[rp_idx.reshape(-1)].reshape(N, N, nh), 2, 0, 1)
    attn = attn + bias[None, :, :, :]
    attn = (attn.reshape(B, nWh * nWw, nh, N, N) + attn_mask[None, :, None, :, :]).reshape(-1, nh, N, N)
    attn = _softmax(attn, axis=-1)
    out = (lambda x, *d: x.permute(*d))(torch.einsum('whij,whjd->whid', attn, v), 0, 2, 1, 3).reshape(-1, N, C)
    out = torch.einsum('wnc,cd->wnd', out, wo)
    out = (lambda x, *d: x.permute(*d))(out.reshape(B, nWh, nWw, ws, ws, C), 0, 1, 3, 2, 4, 5).reshape(B, H, W, C)
    return torch.roll(out, (shift, shift), (1, 2))
```

```cpp
#include <hip/hip_runtime.h>
#include <hip/hip_bf16.h>
#include <stdint.h>

typedef unsigned short u16;
__device__ __forceinline__ float bf2f(u16 v){ return __uint_as_float(((unsigned)v)<<16); }
__device__ __forceinline__ u16 f2bf(float f){ __hip_bfloat16 b = __float2bfloat16(f); return *reinterpret_cast<u16*>(&b); }

__global__ __launch_bounds__(256) void naive_kernel(const u16* __restrict__ x, const u16* __restrict__ wqkv, const u16* __restrict__ wo,
    const u16* __restrict__ rpb, const int* __restrict__ rp_idx, const u16* __restrict__ mask, u16* __restrict__ out)
{
  extern __shared__ float sm[];
  float* xs = sm;
  float* qkv = sm + 64*192;
  u16* qkvb = (u16*)qkv;
  float* S = (float*)(qkvb + 64*576);
  int w = blockIdx.x; int wh = w / 28, ww = w % 28;
  int tid = threadIdx.x;
  for (int e = tid; e < 64*192; e += 256) {
    int n = e / 192, c = e % 192;
    int r = (wh*8 + (n>>3) + 4) % 224, cc = (ww*8 + (n&7) + 4) % 224;
    xs[e] = bf2f(x[(r*224+cc)*192 + c]);
  }
  __syncthreads();
  for (int e = tid; e < 64*576; e += 256) {
    int n = e / 576, d = e % 576;
    float a = 0.f;
    for (int c = 0; c < 192; ++c) a += xs[n*192+c] * bf2f(wqkv[c*576+d]);
    qkvb[e] = f2bf(a);
  }
  __syncthreads();
  for (int h = 0; h < 4; ++h) {
    for (int e = tid; e < 4096; e += 256) {
      int i = e >> 6, j = e & 63;
      float a = 0.f;
      for (int d = 0; d < 48; ++d) a += bf2f(qkvb[i*576 + h*48 + d]) * bf2f(qkvb[j*576 + 192 + h*48 + d]);
      a = a * 0.14433756729740646f + bf2f(rpb[rp_idx[e]*4 + h]) + bf2f(mask[(size_t)w*4096 + e]);
      S[e] = a;
    }
    __syncthreads();
    if (tid < 64) {
      float m = -INFINITY;
      for (int j = 0; j < 64; ++j) m = fmaxf(m, S[tid*64+j]);
      float s = 0.f;
      for (int j = 0; j < 64; ++j) { float p = __expf(S[tid*64+j]-m); S[tid*64+j] = p; s += p; }
      float inv = 1.f/s;
      for (int j = 0; j < 64; ++j) S[tid*64+j] = bf2f(f2bf(S[tid*64+j]*inv));
    }
    __syncthreads();
    for (int e = tid; e < 64*48; e += 256) {
      int i = e / 48, d = e % 48;
      float a = 0.f;
      for (int j = 0; j < 64; ++j) a += S[i*64+j] * bf2f(qkvb[j*576 + 384 + h*48 + d]);
      xs[i*192 + h*48 + d] = bf2f(f2bf(a));
    }
    __syncthreads();
  }
  for (int e = tid; e < 64*192; e += 256) {
    int n = e / 192, c = e % 192;
    float a = 0.f;
    for (int k = 0; k < 192; ++k) a += xs[n*192+k] * bf2f(wo[k*192+c]);
    int r = (wh*8 + (n>>3) + 4) % 224, cc = (ww*8 + (n&7) + 4) % 224;
    out[(r*224+cc)*192 + c] = f2bf(a);
  }
}

extern "C" void kernel_launch(void* const* d_in, const int* in_sizes, int n_in,
                              void* d_out, int out_size, void* d_ws, size_t ws_size,
                              hipStream_t stream) {
  size_t shm = 64*192*4 + 64*576*2 + 4096*4;
  hipFuncSetAttribute((const void*)naive_kernel, hipFuncAttributeMaxDynamicSharedMemorySize, (int)shm);
  naive_kernel<<<784, 256, shm, stream>>>((const u16*)d_in[0], (const u16*)d_in[1], (const u16*)d_in[2], (const u16*)d_in[3], (const int*)d_in[4], (const u16*)d_in[5], (u16*)d_out);
}
```

```cpp
#include <hip/hip_runtime.h>
#include <hip/hip_bf16.h>
#include <stdint.h>

__global__ __launch_bounds__(256, 1) __attribute__((aligned(4096)))
void swin_window_attn_kernel(const void* x, const void* wqkv, const void* wo, const void* rpb, const void* idx, const void* mask,
                             void* out) {
  const unsigned wv = __builtin_amdgcn_readfirstlane(threadIdx.x >> 6);
  asm volatile(
    "s_load_dwordx8 s[4:11], %[ka], 0x0\n"
    "s_load_dwordx4 s[12:15], %[ka], 0x20\n"
    "s_load_dwordx2 s[16:17], %[ka], 0x30\n"
    "s_mov_b32 s69, %[wv]\n"
    "s_mov_b32 s70, %[bid]\n"
    "s_getpc_b64 s[56:57]\n"
    "s_bfe_u32 s71, s70, 0x50003\n"
    "s_movk_i32 s100, 1280\n"
    "s_cmp_eq_u32 s69, 2\n"
    "s_cselect_b32 s100, 2304, s100\n"
    "s_cmp_lt_u32 s69, 2\n"
    "s_cselect_b32 s100, 6912, s100\n"
    "s_mul_i32 s101, s71, s100\n"
    "s_cmp_eq_u32 s69, 1\n"
    "s_cselect_b32 s71, 4096, 0\n"
    "s_add_u32 s101, s101, s71\n"
    "s_sub_u32 s58, s100, s71\n"
    "s_cmp_eq_u32 s69, 3\n"
    "s_cselect_b32 s100, %[code], -1\n"
    "s_cmp_lt_u32 s101, s100\n"
    "s_cselect_b32 s58, s58, 0\n"
    "s_mov_b32 s59, 0x20000\n"
    "v_mbcnt_lo_u32_b32 v255, -1, 0\n"
    "s_lshl_b32 s67, s69, 9\n"
    "v_mbcnt_hi_u32_b32 v255, -1, v255\n"
    "s_mov_b32 s62, 1800\n"
    "v_lshlrev_b32 v254, 6, v255\n"
    "v_lshl_add_u32 v253, v255, 3, s67\n"
    "s_mov_b32 s63, 0x20000\n"
    "s_add_u32 s96, s70, 0\n"
    "s_mul_i32 s77, s96, 2341\n"
    "s_lshr_b32 s77, s77, 16\n"
    "s_mul_i32 s92, s77, 28\n"
    "s_sub_u32 s92, s96, s92\n"
    "s_mul_i32 s79, s92, 3072\n"
    "s_mul_i32 s76, s77, 688128\n"
    "s_add_u32 s92, s79, 1536\n"
    "s_add_u32 s79, s79, 3072\n"
    "s_add_u32 s78, s76, 688128\n"
    "s_cmp_eq_u32 s79, 86016\n"
    "s_cselect_b32 s79, 0, s79\n"
    "v_lshrrev_b32 v252, 4, v255\n"
    "v_lshrrev_b32 v251, 5, v255\n"
    "s_cmp_eq_u32 s77, 27\n"
    "s_cselect_b32 s78, 0, s78\n"
    "s_add_u32 s76, s76, 344064\n"
    "s_add_u32 s77, s76, s79\n"
    "v_bfe_u32 v250, v255, 3, 2\n"
    "v_xor_b32 v249, v252, v255\n"
    "v_mul_u32_u24 v248, 0xe0, v251\n"
    "s_add_u32 s79, s78, s79\n"
    "s_add_u32 s76, s76, s92\n"
    "s_add_u32 s78, s78, s92\n"
    "v_and_b32 v249, 7, v249\n"
    "v_add_u32 v248, v248, v250\n"
    "s_bitcmp1_b32 s69, 0\n"
    "s_cselect_b32 s92, s77, s76\n"
    "s_cselect_b32 vcc_lo, s79, s78\n"
    "s_bitcmp1_b32 s69, 1\n"
    "s_cselect_b32 s92, vcc_lo, s92\n"
    "v_lshlrev_b32 v249, 4, v249\n"
    "v_mul_u32_u24 v248, 0x180, v248\n"
    "s_lshl_b32 s72, s69, 11\n"
    "v_add_u32 v216, v248, v249\n"
    "s_mov_b32 s38, 19267584\n"
    "s_mov_b32 s39, 0x20000\n"
    "s_mov_b32 m0, s72\n"
    "v_xor_b32 v217, 64, v216\n"
    "v_mul_u32_u24 v248, 0x2aab, v255\n"
    "s_waitcnt lgkmcnt(0)\n"
    "s_cmp_eq_u32 s69, 2\n"
    "s_cselect_b32 s56, s8, s56\n"
    "s_cselect_b32 s57, s9, s57\n"
    "s_cmp_lt_u32 s69, 2\n"
    "s_cselect_b32 s56, s6, s56\n"
    "s_cselect_b32 s57, s7, s57\n"
    "s_add_u32 s56, s56, s101\n"
    "s_addc_u32 s57, s57, 0\n"
    "s_and_b32 s57, s57, 0xffff\n"
    "buffer_load_dword v204, v254, s[56:59], 0 offen\n"
    "s_mov_b32 s60, s10\n"
    "s_and_b32 s61, s11, 0xffff\n"
    "buffer_load_dwordx2 v[0:1], v253, s[60:63], 0 offen offset:0\n"
    "s_mov_b32 s36, s4\n"
    "s_and_b32 s37, s5, 0xffff\n"
    "s_cmp_lt_u32 s70, 64\n"
    "s_cbranch_scc1 LNOD%=\n"
    "s_sleep 32\n"
    "LNOD%=:\n"
    "buffer_load_dwordx4 v216, s[36:39], s92 offen offset:0 nt lds\n"
    "v_add_u32 v217, 171008, v217\n"
    "s_add_u32 s101, s92, 128\n"
    "buffer_load_dwordx4 v217, s[36:39], s92 offen offset:1024 nt lds\n"
    "s_add_u32 m0, s72, 8192\n"
    "v_lshrrev_b32 v248, 16, v248\n"
    "buffer_load_dwordx4 v216, s[36:39], s101 offen offset:0 nt lds\n"
    "buffer_load_dwordx4 v217, s[36:39], s101 offen offset:1024 nt lds\n"
    "v_and_b32 v254, 19, v248\n"
    "v_and_b32 v247, 4, v248\n"
    "v_mul_u32_u24 v246, 6, v248\n"
    "v_lshl_or_b32 v254, v247, 1, v254\n"
    "v_bfe_u32 v247, v248, 3, 1\n"
    "v_sub_u32 v246, v255, v246\n"
    "v_lshl_or_b32 v254, v247, 2, v254\n"
    "s_mul_i32 s74, s69, 9216\n"
    "v_lshlrev_b32 v246, 4, v246\n"
    "v_mul_u32_u24 v247, 0x480, v254\n"
    "s_add_u32 s74, s74, 57344\n"
    "s_mul_i32 s35, s69, 96\n"
    "v_add_u32 v248, v247, v246\n"
    "s_mov_b32 s48, s6\n"
    "s_and_b32 s49, s7, 0xffff\n"
    "s_mov_b32 s50, 221184\n"
    "s_mov_b32 s51, 0x20000\n"
    "s_mov_b32 m0, s74\n"
    "s_add_u32 s100, s35, 384\n"
    "buffer_load_dwordx4 v248, s[48:51], s100 offen offset:0 lds\n"
    "v_add_u32 v247, 64, v255\n"
    "v_add_u32 v245, 128, v255\n"
    "v_mul_u32_u24 v244, 0x2aab, v247\n"
    "v_mul_u32_u24 v243, 0x2aab, v245\n"
    "v_lshrrev_b32 v244, 16, v244\n"
    "v_lshrrev_b32 v243, 16, v243\n"
    "v_and_b32 v242, 19, v244\n"
    "v_and_b32 v241, 4, v244\n"
    "v_mul_u32_u24 v240, 6, v244\n"
    "v_lshl_or_b32 v242, v241, 1, v242\n"
    "v_sub_u32 v240, v247, v240\n"
    "v_bfe_u32 v241, v244, 3, 1\n"
    "v_lshlrev_b32 v240, 4, v240\n"
    "v_lshl_or_b32 v242, v241, 2, v242\n"
    "v_subrev_u32 v240, 1024, v240\n"
    "v_mul_u32_u24 v241, 0x480, v242\n"
    "v_and_b32 v247, 19, v243\n"
    "v_add_u32 v244, v241, v240\n"
    "v_and_b32 v241, 4, v243\n"
    "buffer_load_dwordx4 v244, s[48:51], s100 offen offset:1024 lds\n"
    "v_mul_u32_u24 v239, 6, v243\n"
    "v_lshl_or_b32 v247, v241, 1, v247\n"
    "v_sub_u32 v239, v245, v239\n"
    "v_bfe_u32 v241, v243, 3, 1\n"
    "v_lshlrev_b32 v239, 4, v239\n"
    "v_lshl_or_b32 v247, v241, 2, v247\n"
    "v_subrev_u32 v239, 2048, v239\n"
    "v_mul_u32_u24 v241, 0x480, v247\n"
    "s_lshr_b32 s28, s69, 1\n"
    "v_add_u32 v245, v241, v239\n"
    "s_and_b32 s29, s69, 1\n"
    "buffer_load_dwordx4 v245, s[48:51], s100 offen offset:2048 lds\n"
    "s_lshl_b32 s28, s28, 5\n"
    "s_lshl_b32 s29, s29, 2\n"
    "s_add_u32 s28, s28, s29\n"
    "v_lshlrev_b32 v243, 4, v255\n"
    "s_lshl_b32 s34, s28, 8\n"
    "s_mov_b32 s24, s12\n"
    "s_and_b32 s25, s13, 0xffff\n"
    "s_mov_b32 s26, 16384\n"
    "s_mov_b32 s27, 0x20000\n"
    "buffer_load_dwordx4 v[136:139], v243, s[24:27], s34 offen offset:0\n"
    "s_add_u32 s67, s34, 4096\n"
    "s_add_u32 m0, s74, 3072\n"
    "s_add_u32 s100, s35, 0\n"
    "s_mul_i32 s66, s69, 3072\n"
    "s_add_u32 s66, s66, 45056\n"
    "s_add_u32 s101, s92, 256\n"
    "s_lshl_b32 s73, s69, 11\n"
    "v_lshl_add_u32 v251, v251, 3, v250\n"
    "s_add_u32 s73, s73, 24576\n"
    "s_lshl_b32 s28, s28, 7\n"
    "s_lshl_b32 s96, s96, 13\n"
    "v_lshl_add_u32 v218, v251, 7, v249\n"
    "s_add_u32 s96, s96, s28\n"
    "s_mov_b32 s40, s14\n"
    "s_and_b32 s41, s15, 0xffff\n"
    "s_mov_b32 s42, 6422528\n"
    "s_mov_b32 s43, 0x20000\n"
    "v_xor_b32 v219, 64, v218\n"
    "s_add_u32 s97, s70, 256\n"
    "v_add_u32 v219, 1024, v219\n"
    "s_mul_i32 s81, s97, 2341\n"
    "s_lshr_b32 s81, s81, 16\n"
    "s_mul_i32 s93, s81, 28\n"
    "s_sub_u32 s93, s97, s93\n"
    "s_mul_i32 s83, s93, 3072\n"
    "s_mul_i32 s80, s81, 688128\n"
    "s_add_u32 s93, s83, 1536\n"
    "s_add_u32 s83, s83, 3072\n"
    "s_add_u32 s82, s80, 688128\n"
    "s_cmp_eq_u32 s83, 86016\n"
    "s_cselect_b32 s83, 0, s83\n"
    "s_cmp_eq_u32 s81, 27\n"
    "s_cselect_b32 s82, 0, s82\n"
    "s_add_u32 s80, s80, 344064\n"
    "s_add_u32 s81, s80, s83\n"
    "s_add_u32 s83, s82, s83\n"
    "s_add_u32 s80, s80, s93\n"
    "s_add_u32 s82, s82, s93\n"
    "s_bitcmp1_b32 s69, 0\n"
    "s_cselect_b32 s93, s81, s80\n"
    "s_cselect_b32 vcc_lo, s83, s82\n"
    "s_bitcmp1_b32 s69, 1\n"
    "s_cselect_b32 s93, vcc_lo, s93\n"
    "v_lshrrev_b32 v251, 1, v252\n"
    "v_and_b32 v250, 15, v255\n"
    "v_and_b32 v249, 1, v252\n"
    "v_lshlrev_b32 v251, 4, v251\n"
    "v_lshrrev_b32 v241, 2, v250\n"
    "v_lshl_add_u32 v251, v249, 2, v251\n"
    "v_and_b32 v238, 3, v255\n"
    "v_add_u32 v251, v251, v241\n"
    "s_lshl_b32 s32, s69, 5\n"
    "v_mul_u32_u24 v249, 0x60, v251\n"
    "s_lshl_b32 s31, s69, 6\n"
    "v_lshl_add_u32 v251, v238, 3, v249\n"
    "s_add_u32 s32, s32, 192\n"
    "v_add_u32 v201, s74, v251\n"
    "v_mov_b32 v237, s32\n"
    "v_mov_b32 v236, s31\n"
    "v_mul_u32_u24 v254, 0x180, v254\n"
    "v_cmp_gt_i32 vcc, 64, v246\n"
    "s_nop 1\n"
    "v_cndmask_b32 v237, v237, v236, vcc\n"
    "v_add_u32 v254, v254, v246\n"
    "s_mov_b32 s52, s8\n"
    "v_add_u32 v254, v254, v237\n"
    "s_and_b32 s53, s9, 0xffff\n"
    "s_mov_b32 s54, 73728\n"
    "s_mov_b32 s55, 0x20000\n"
    "v_mov_b32 v246, s32\n"
    "v_mov_b32 v237, s31\n"
    "v_mul_u32_u24 v242, 0x180, v242\n"
    "v_cmp_gt_i32 vcc, -960, v240\n"
    "s_nop 1\n"
    "v_cndmask_b32 v246, v246, v237, vcc\n"
    "v_add_u32 v242, v242, v240\n"
    "v_mov_b32 v240, s32\n"
    "v_add_u32 v246, v242, v246\n"
    "v_mov_b32 v242, s31\n"
    "v_mul_u32_u24 v247, 0x180, v247\n"
    "v_cmp_gt_i32 vcc, -1984, v239\n"
    "s_nop 1\n"
    "v_cndmask_b32 v240, v240, v242, vcc\n"
    "v_add_u32 v247, v247, v239\n"
    "v_bfe_u32 v242, v255, 1, 3\n"
    "v_add_u32 v247, v247, v240\n"
    "v_xor_b32 v240, v252, v242\n"
    "v_lshlrev_b32 v239, 7, v250\n"
    "v_lshlrev_b32 v253, 1, v253\n"
    "v_lshl_add_u32 v205, v240, 4, v239\n"
    "v_add_u32 v253, 94208, v253\n"
    "v_and_b32 v240, 7, v255\n"
    "v_bfe_u32 v237, v255, 1, 2\n"
    "v_xor_b32 v240, v240, v252\n"
    "s_lshl_b32 s75, s69, 12\n"
    "v_lshlrev_b32 v240, 4, v240\n"
    "s_add_u32 s75, s75, 98304\n"
    "v_lshl_add_u32 v240, v237, 8, v240\n"
    "v_and_b32 v237, 1, v255\n"
    "v_lshl_add_u32 v249, v238, 4, v249\n"
    "v_lshl_add_u32 v240, v237, 10, v240\n"
    "v_bfe_u32 v237, v255, 3, 1\n"
    "v_add_u32 v255, s74, v249\n"
    "v_lshl_add_u32 v240, v237, 11, v240\n"
    "v_add_u32 v251, s66, v251\n"
    "v_add_u32 v249, s75, v240\n"
    "v_xor_b32 v206, 64, v205\n"
    "v_xor_b32 v240, 64, v249\n"
    "v_lshlrev_b32 v237, 5, v252\n"
    "s_lshl_b32 s30, s69, 14\n"
    "v_xor_b32 v237, v237, v243\n"
    "s_add_u32 s30, s30, 98304\n"
    "v_add_u32 v209, s30, v237\n"
    "v_lshlrev_b32 v237, 2, v252\n"
    "v_xor_b32 v210, 16, v209\n"
    "v_sub_u32 v237, v250, v237\n"
    "v_mov_b32 v250, 0x40de\n"
    "v_mov_b32 v236, 0x40de0000\n"
    "v_cmp_eq_u32 vcc, 0, v237\n"
    "s_nop 1\n"
    "v_cndmask_b32 v235, 0, v250, vcc\n"
    "v_cmp_eq_u32 vcc, 1, v237\n"
    "s_nop 1\n"
    "v_cndmask_b32 v54, v235, v236, vcc\n"
    "v_cmp_eq_u32 vcc, 2, v237\n"
    "s_nop 1\n"
    "v_cndmask_b32 v235, 0, v250, vcc\n"
    "v_cmp_eq_u32 vcc, 3, v237\n"
    "s_nop 1\n"
    "v_cndmask_b32 v55, v235, v236, vcc\n"
    "v_mov_b32 v60, v54\n"
    "v_mov_b32 v61, v55\n"
    "v_add_u32 v207, 0x10000, v205\n"
    "v_add_u32 v208, 0x10000, v206\n"
    "s_mov_b32 s71, 0xbe553b95\n"
    "s_mov_b32 s68, 0x3e553b95\n"
    "v_mov_b32 v220, 0x3f803f80\n"
    "v_mov_b32 v221, 0x3f803f80\n"
    "v_mov_b32 v222, 0x3f803f80\n"
    "v_mov_b32 v223, 0x3f803f80\n"
    "v_lshrrev_b32 v250, 1, v252\n"
    "s_mul_i32 s29, s69, 6\n"
    "v_add_u32 v250, s29, v250\n"
    "v_and_b32 v237, 1, v252\n"
    "v_add_u32 v236, 0, v250\n"
    "v_add_u32 v235, 2, v250\n"
    "v_add_u32 v250, 4, v250\n"
    "v_lshl_add_u32 v237, v237, 3, v239\n"
    "v_lshrrev_b32 v239, 3, v236\n"
    "v_lshrrev_b32 v234, 3, v235\n"
    "v_lshrrev_b32 v233, 3, v250\n"
    "v_add_u32 v237, 0x10000, v237\n"
    "v_xor_b32 v236, v236, v242\n"
    "v_xor_b32 v235, v235, v242\n"
    "v_xor_b32 v250, v250, v242\n"
    "v_lshl_add_u32 v239, v239, 13, v237\n"
    "v_and_b32 v236, 7, v236\n"
    "v_lshl_add_u32 v234, v234, 13, v237\n"
    "v_and_b32 v235, 7, v235\n"
    "v_lshl_add_u32 v233, v233, 13, v237\n"
    "v_and_b32 v250, 7, v250\n"
    "v_lshl_add_u32 v211, v236, 4, v239\n"
    "v_lshl_add_u32 v212, v235, 4, v234\n"
    "v_lshl_add_u32 v213, v250, 4, v233\n"
    "s_cmp_lt_u32 s70, 64\n"
    "s_cselect_b32 s33, 1, 0\n"
    "s_lshl_b32 s97, s97, 13\n"
    "s_add_u32 s97, s97, s28\n"
    "s_add_u32 s98, s70, 512\n"
    "s_mul_i32 s85, s98, 2341\n"
    "s_lshr_b32 s85, s85, 16\n"
    "s_mul_i32 s94, s85, 28\n"
    "s_sub_u32 s94, s98, s94\n"
    "s_mul_i32 s87, s94, 3072\n"
    "s_mul_i32 s84, s85, 688128\n"
    "s_add_u32 s94, s87, 1536\n"
    "s_and_b32 s99, s70, 7\n"
    "s_add_u32 s87, s87, 3072\n"
    "s_add_u32 s86, s84, 688128\n"
    "s_lshl_b32 s99, s99, 1\n"
    "s_bfe_u32 s89, s70, 0x10005\n"
    "s_cmp_eq_u32 s87, 86016\n"
    "s_cselect_b32 s87, 0, s87\n"
    "s_cmp_eq_u32 s85, 27\n"
    "s_cselect_b32 s86, 0, s86\n"
    "s_add_u32 s84, s84, 344064\n"
    "s_add_u32 s99, s99, s89\n"
    "s_add_u32 s85, s84, s87\n"
    "s_add_u32 s99, s99, 768\n"
    "s_add_u32 s87, s86, s87\n"
    "s_add_u32 s84, s84, s94\n"
    "s_add_u32 s86, s86, s94\n"
    "s_mul_i32 s89, s99, 2341\n"
    "s_bitcmp1_b32 s69, 0\n"
    "s_cselect_b32 s94, s85, s84\n"
    "s_cselect_b32 vcc_lo, s87, s86\n"
    "s_bitcmp1_b32 s69, 1\n"
    "s_cselect_b32 s94, vcc_lo, s94\n"
    "s_lshr_b32 s89, s89, 16\n"
    "s_mul_i32 s95, s89, 28\n"
    "s_sub_u32 s95, s99, s95\n"
    "v_mul_u32_u24 v250, 0xe0, v241\n"
    "s_mul_i32 s91, s95, 3072\n"
    "s_mul_i32 s88, s89, 688128\n"
    "s_add_u32 s95, s91, 1536\n"
    "v_add_u32 v250, v250, v238\n"
    "s_add_u32 s91, s91, 3072\n"
    "s_add_u32 s90, s88, 688128\n"
    "v_mul_u32_u24 v250, 0x180, v250\n"
    "s_cmp_eq_u32 s91, 86016\n"
    "s_cselect_b32 s91, 0, s91\n"
    "s_cmp_eq_u32 s89, 27\n"
    "s_cselect_b32 s90, 0, s90\n"
    "s_add_u32 s88, s88, 344064\n"
    "v_lshl_add_u32 v242, v252, 3, v250\n"
    "s_add_u32 s89, s88, s91\n"
    "s_add_u32 s91, s90, s91\n"
    "v_lshl_add_u32 v252, v252, 4, v250\n"
    "v_add_u32 v242, s32, v242\n"
    "s_lshl_b32 s98, s98, 13\n"
    "s_add_u32 s88, s88, s95\n"
    "s_add_u32 s90, s90, s95\n"
    "s_lshl_b32 s99, s99, 13\n"
    "s_bfe_u32 s64, s70, 0x20003\n"
    "s_add_u32 s98, s98, s28\n"
    "s_bitcmp1_b32 s69, 0\n"
    "s_cselect_b32 s95, s89, s88\n"
    "s_cselect_b32 vcc_lo, s91, s90\n"
    "s_bitcmp1_b32 s69, 1\n"
    "s_cselect_b32 s95, vcc_lo, s95\n"
    "s_bitcmp1_b32 s70, 3\n"
    "s_cselect_b32 s65, s89, s88\n"
    "s_cselect_b32 vcc_lo, s91, s90\n"
    "s_bitcmp1_b32 s70, 4\n"
    "s_cselect_b32 s65, vcc_lo, s65\n"
    "buffer_load_dwordx4 v[192:195], v243, s[24:27], s34 offen offset:2048\n"
    "s_add_u32 s99, s99, s28\n"
    "s_lshl_b32 s64, s64, 11\n"
    "s_mov_b32 s44, s16\n"
    "s_and_b32 s45, s17, 0xffff\n"
    "s_mov_b32 s46, 19267584\n"
    "s_mov_b32 s47, 0x20000\n"
    "v_add_u32 v214, s31, v252\n"
    "v_add_u32 v215, 64, v242\n"
    "buffer_load_dwordx4 v[68:71], v243, s[24:27], s67 offen offset:0\n"
    "s_waitcnt vmcnt(10)\n"
    "v_lshlrev_b32 v16, 16, v0\n"
    "v_and_b32 v17, 0xffff0000, v0\n"
    "v_lshlrev_b32 v18, 16, v1\n"
    "v_and_b32 v19, 0xffff0000, v1\n"
    "v_mul_f32 v16, 0x40ddb3d7, v16\n"
    "v_mul_f32 v17, 0x40ddb3d7, v17\n"
    "v_mul_f32 v18, 0x40ddb3d7, v18\n"
    "v_mul_f32 v19, 0x40ddb3d7, v19\n"
    "ds_write_b128 v253, v[16:19]\n"
    "s_waitcnt lgkmcnt(0)\n"
    "s_barrier\n"
    "buffer_load_dwordx4 v[224:227], v243, s[24:27], s67 offen offset:2048\n"
    "buffer_load_dwordx4 v248, s[48:51], s100 offen offset:0 lds\n"
    "s_mov_b32 s24, 94208\n"
    "buffer_load_dwordx4 v244, s[48:51], s100 offen offset:1024 lds\n"
    "s_waitcnt vmcnt(9)\n"
    "s_barrier\n"
    "buffer_load_dwordx4 v245, s[48:51], s100 offen offset:2048 lds\n"
    "s_add_u32 m0, s74, 6144\n"
    "s_add_u32 s100, s35, 37248\n"
    "ds_read_b128 v[0:3], v205 offset:0\n"
    "ds_read_b128 v[16:19], v205 offset:2048\n"
    "buffer_load_dwordx4 v248, s[48:51], s100 offen offset:0 lds\n"
    "ds_read_b128 v[124:127], v205 offset:4096\n"
    "ds_read_b128 v[140:143], v205 offset:6144\n"
    "buffer_load_dwordx4 v244, s[48:51], s100 offen offset:1024 lds\n"
    "buffer_load_dwordx4 v245, s[48:51], s100 offen offset:2048 lds\n"
    "s_mov_b32 m0, s66\n"
    "s_add_u32 s100, s35, 36864\n"
    "s_waitcnt vmcnt(10)\n"
    "ds_read_b64_tr_b16 a[0:1], v201 offset:0\n"
    "ds_read_b64_tr_b16 a[2:3], v201 offset:768\n"
    "ds_read_b64_tr_b16 a[24:25], v201 offset:32\n"
    "ds_read_b64_tr_b16 a[26:27], v201 offset:800\n"
    "ds_read_b64_tr_b16 a[48:49], v201 offset:64\n"
    "ds_read_b64_tr_b16 a[50:51], v201 offset:832\n"
    "buffer_load_dwordx4 v248, s[48:51], s100 offen offset:0 lds\n"
    "s_waitcnt vmcnt(10)\n"
    "v_lshl_add_u32 v136, v136, 4, s24\n"
    "v_lshl_add_u32 v137, v137, 4, s24\n"
    "v_lshl_add_u32 v138, v138, 4, s24\n"
    "v_lshl_add_u32 v139, v139, 4, s24\n"
    "ds_read_b128 v[160:163], v136 offset:0\n"
    "ds_read_b128 v[20:23], v137 offset:0\n"
    "ds_read_b128 v[24:27], v138 offset:0\n"
    "ds_read_b128 v[56:59], v139 offset:0\n"
    "s_waitcnt vmcnt(9)\n"
    "v_lshl_add_u32 v192, v192, 4, s24\n"
    "v_lshl_add_u32 v193, v193, 4, s24\n"
    "s_waitcnt lgkmcnt(8)\n"
    "v_mfma_f32_16x16x32_bf16 v[180:183], a[0:3], v[0:3], 0\n"
    "buffer_load_dwordx4 v244, s[48:51], s100 offen offset:1024 lds\n"
    "v_mfma_f32_16x16x32_bf16 v[144:147], a[0:3], v[16:19], 0\n"
    "v_lshl_add_u32 v194, v194, 4, s24\n"
    "v_lshl_add_u32 v195, v195, 4, s24\n"
    "v_mfma_f32_16x16x32_bf16 v[196:199], a[0:3], v[124:127], 0\n"
    "ds_read_b128 v[64:67], v192 offset:0\n"
    "v_mfma_f32_16x16x32_bf16 v[4:7], a[0:3], v[140:143], 0\n"
    "ds_read_b128 v[228:231], v193 offset:0\n"
    "s_waitcnt vmcnt(9)\n"
    "v_lshl_add_u32 v68, v68, 4, s24\n"
    "s_waitcnt lgkmcnt(8)\n"
    "v_mfma_f32_16x16x32_bf16 v[232:235], a[24:27], v[0:3], 0\n"
    "buffer_load_dwordx4 v245, s[48:51], s100 offen offset:2048 lds\n"
    "v_mfma_f32_16x16x32_bf16 v[236:239], a[24:27], v[16:19], 0\n"
    "s_add_u32 m0, s72, 16384\n"
    "s_xor_b32 s72, s72, 0x8000\n"
    "s_add_u32 s100, s35, 74112\n"
    "v_mfma_f32_16x16x32_bf16 v[104:107], a[24:27], v[124:127], 0\n"
    "v_lshl_add_u32 v69, v69, 4, s24\n"
    "v_lshl_add_u32 v70, v70, 4, s24\n"
    "v_mfma_f32_16x16x32_bf16 v[8:11], a[24:27], v[140:143], 0\n"
    "s_waitcnt lgkmcnt(6)\n"
    "v_lshl_add_u32 v71, v71, 4, s24\n"
    "v_mfma_f32_16x16x32_bf16 v[120:123], a[48:51], v[0:3], 0\n"
    "buffer_load_dwordx4 v216, s[36:39], s101 offen offset:0 nt lds\n"
    "v_mfma_f32_16x16x32_bf16 v[28:31], a[48:51], v[16:19], 0\n"
    "s_waitcnt vmcnt(10)\n"
    "v_lshl_add_u32 v224, v224, 4, s24\n"
    "v_lshl_add_u32 v225, v225, 4, s24\n"
    "v_mfma_f32_16x16x32_bf16 v[72:75], a[48:51], v[124:127], 0\n"
    "v_lshl_add_u32 v226, v226, 4, s24\n"
    "v_lshl_add_u32 v227, v227, 4, s24\n"
    "v_mfma_f32_16x16x32_bf16 v[76:79], a[48:51], v[140:143], 0\n"
    "ds_read_b128 v[80:83], v194 offset:0\n"
    "ds_read_b128 v[32:35], v195 offset:0\n"
    "buffer_load_dwordx4 v217, s[36:39], s101 offen offset:1024 nt lds\n"
    "s_mov_b32 m0, s73\n"
    "s_add_u32 s101, s93, 128\n"
    "ds_read_b128 v[188:191], v68 offset:0\n"
    "ds_read_b128 v[84:87], v69 offset:0\n"
    "ds_read_b128 v[12:15], v70 offset:0\n"
    "ds_read_b128 v[36:39], v71 offset:0\n"
    "ds_read_b128 v[192:195], v224 offset:0\n"
    "ds_read_b128 v[40:43], v225 offset:0\n"
    "buffer_load_dwordx4 v218, s[40:43], s96 offen offset:0 nt lds\n"
    "ds_read_b128 v[148:151], v226 offset:0\n"
    "ds_read_b128 v[128:131], v227 offset:0\n"
    "s_waitcnt lgkmcnt(10)\n"
    "v_swap_b32 v161, v20\n"
    "v_swap_b32 v162, v24\n"
    "v_swap_b32 v163, v56\n"
    "v_swap_b32 v22, v25\n"
    "v_swap_b32 v23, v57\n"
    "buffer_load_dwordx4 v219, s[40:43], s96 offen offset:1024 nt lds\n"
    "s_mov_b32 m0, s72\n"
    "s_waitcnt vmcnt(10)\n"
    "ds_read_b64_tr_b16 a[72:73], v201 offset:3072\n"
    "ds_read_b64_tr_b16 a[74:75], v201 offset:3840\n"
    "ds_read_b64_tr_b16 a[96:97], v201 offset:3104\n"
    "ds_read_b64_tr_b16 a[98:99], v201 offset:3872\n"
    "ds_read_b64_tr_b16 a[120:121], v201 offset:3136\n"
    "ds_read_b64_tr_b16 a[122:123], v201 offset:3904\n"
    "buffer_load_dwordx4 v216, s[36:39], s93 offen offset:0 nt lds\n"
    "v_swap_b32 v27, v58\n"
    "v_swap_b32 v65, v228\n"
    "s_waitcnt lgkmcnt(11)\n"
    "v_swap_b32 v66, v80\n"
    "v_swap_b32 v67, v32\n"
    "v_swap_b32 v230, v81\n"
    "v_swap_b32 v231, v33\n"
    "v_swap_b32 v83, v34\n"
    "v_swap_b32 v189, v84\n"
    "s_waitcnt lgkmcnt(4)\n"
    "v_mfma_f32_16x16x32_bf16 v[68:71], a[72:75], v[0:3], 0\n"
    "buffer_load_dwordx4 v217, s[36:39], s93 offen offset:1024 nt lds\n"
    "v_mfma_f32_16x16x32_bf16 v[108:111], a[72:75], v[16:19], 0\n"
    "s_mov_b32 m0, s74\n"
    "v_swap_b32 v190, v12\n"
    "v_mfma_f32_16x16x32_bf16 v[88:91], a[72:75], v[124:127], 0\n"
    "v_swap_b32 v191, v36\n"
    "v_mfma_f32_16x16x32_bf16 v[92:95], a[72:75], v[140:143], 0\n"
    "ds_write_b128 v249, v[160:163] offset:0\n"
    "s_waitcnt lgkmcnt(1)\n"
    "v_mfma_f32_16x16x32_bf16 v[96:99], a[96:99], v[0:3], 0\n"
    "buffer_load_dwordx4 v248, s[48:51], s100 offen offset:0 lds\n"
    "v_mfma_f32_16x16x32_bf16 v[100:103], a[96:99], v[16:19], 0\n"
    "buffer_load_dwordx4 v244, s[48:51], s100 offen offset:1024 lds\n"
    "v_mfma_f32_16x16x32_bf16 v[224:227], a[96:99], v[124:127], 0\n"
    "s_waitcnt vmcnt(11)\n"
    "ds_read_b64_tr_b16 a[4:5], v201 offset:6144\n"
    "s_xor_b32 s73, s73, 0x8000\n"
    "v_mfma_f32_16x16x32_bf16 v[112:115], a[96:99], v[140:143], 0\n"
    "ds_read_b64_tr_b16 a[6:7], v201 offset:6912\n"
    "v_mfma_f32_16x16x32_bf16 v[116:119], a[120:123], v[0:3], 0\n"
    "ds_write_b128 v249, v[20:23] offset:16384\n"
    "v_mfma_f32_16x16x32_bf16 v[132:135], a[120:123], v[16:19], 0\n"
    "buffer_load_dwordx4 v245, s[48:51], s100 offen offset:2048 lds\n"
    "v_mfma_f32_16x16x32_bf16 v[0:3], a[120:123], v[124:127], 0\n"
    "s_add_u32 m0, s74, 3072\n"
    "s_add_u32 s100, s35, 73728\n"
    "v_mfma_f32_16x16x32_bf16 v[124:127], a[120:123], v[140:143], 0\n"
    "buffer_load_dwordx4 v248, s[48:51], s100 offen offset:0 lds\n"
    "ds_read_b64_tr_b16 a[28:29], v201 offset:6176\n"
    "ds_read_b64_tr_b16 a[30:31], v201 offset:6944\n"
    "ds_read_b64_tr_b16 a[52:53], v201 offset:6208\n"
    "ds_read_b64_tr_b16 a[54:55], v201 offset:6976\n"
    "v_swap_b32 v86, v13\n"
    "v_swap_b32 v87, v37\n"
    "v_swap_b32 v15, v38\n"
    "v_swap_b32 v193, v40\n"
    "v_swap_b32 v194, v148\n"
    "v_swap_b32 v195, v128\n"
    "v_swap_b32 v42, v149\n"
    "v_swap_b32 v43, v129\n"
    "v_swap_b32 v151, v130\n"
    "ds_write_b128 v249, v[24:27] offset:32768\n"
    "ds_write_b128 v249, v[56:59] offset:49152\n"
    "ds_write_b128 v240, v[64:67] offset:0\n"
    "ds_write_b128 v240, v[228:231] offset:16384\n"
    "ds_write_b128 v240, v[80:83] offset:32768\n"
    "ds_write_b128 v240, v[32:35] offset:49152\n"
    "ds_write_b128 v249, v[188:191] offset:128\n"
    "ds_write_b128 v249, v[84:87] offset:16512\n"
    "ds_write_b128 v249, v[12:15] offset:32896\n"
    "ds_write_b128 v249, v[36:39] offset:49280\n"
    "ds_write_b128 v240, v[192:195] offset:128\n"
    "ds_write_b128 v240, v[40:43] offset:16512\n"
    "buffer_load_dwordx4 v244, s[48:51], s100 offen offset:1024 lds\n"
    "ds_write_b128 v240, v[148:151] offset:32896\n"
    "ds_write_b128 v240, v[128:131] offset:49280\n"
    "s_waitcnt vmcnt(11)\n"
    "ds_read_b64_tr_b16 a[76:77], v251 offset:0\n"
    "buffer_load_dwordx4 v245, s[48:51], s100 offen offset:2048 lds\n"
    "s_add_u32 m0, s74, 6144\n"
    "s_add_u32 s100, s35, 110976\n"
    "ds_read_b64_tr_b16 a[78:79], v251 offset:768\n"
    "ds_read_b64_tr_b16 a[100:101], v251 offset:32\n"
    "ds_read_b64_tr_b16 a[102:103], v251 offset:800\n"
    "ds_read_b64_tr_b16 a[124:125], v251 offset:64\n"
    "ds_read_b64_tr_b16 a[126:127], v251 offset:832\n"
    "ds_read_b128 v[228:231], v206 offset:0\n"
    "ds_read_b128 v[148:151], v206 offset:2048\n"
    "ds_read_b128 v[128:131], v206 offset:4096\n"
    "ds_read_b128 v[136:139], v206 offset:6144\n"
    "s_mov_b64 exec, 0\n"
    "s_lshl_b32 vcc_lo, s33, 2\n"
    "s_add_u32 vcc_lo, vcc_lo, s69\n"
    "s_cmp_eq_u32 vcc_lo, 0\n"
    "s_cbranch_scc1 LPG0%=\n"
    "s_cmp_eq_u32 vcc_lo, 1\n"
    "s_cbranch_scc1 LPG1%=\n"
    "s_cmp_eq_u32 vcc_lo, 2\n"
    "s_cbranch_scc1 LPG2%=\n"
    "s_cmp_eq_u32 vcc_lo, 3\n"
    "s_cbranch_scc1 LPG3%=\n"
    "s_cmp_eq_u32 vcc_lo, 4\n"
    "s_cbranch_scc1 LPG0%=\n"
    "s_cmp_eq_u32 vcc_lo, 5\n"
    "s_cbranch_scc1 LPG1%=\n"
    "s_cmp_eq_u32 vcc_lo, 6\n"
    "s_cbranch_scc1 LPG4%=\n"
    "s_cmp_eq_u32 vcc_lo, 7\n"
    "s_cbranch_scc1 LPG5%=\n"
    "LWB%=:\n"
    "s_mov_b64 exec, -1\n"
    "s_nop 3\n"
    "s_waitcnt lgkmcnt(3)\n"
    "v_mfma_f32_16x16x32_bf16 v[180:183], a[4:7], v[228:231], v[180:183]\n"
    "s_waitcnt lgkmcnt(2)\n"
    "v_mfma_f32_16x16x32_bf16 v[144:147], a[4:7], v[148:151], v[144:147]\n"
    "buffer_load_dwordx4 v248, s[48:51], s100 offen offset:0 lds\n"
    "s_waitcnt lgkmcnt(1)\n"
    "v_mfma_f32_16x16x32_bf16 v[196:199], a[4:7], v[128:131], v[196:199]\n"
    "s_waitcnt lgkmcnt(0)\n"
    "v_mfma_f32_16x16x32_bf16 v[4:7], a[4:7], v[136:139], v[4:7]\n"
    "v_mfma_f32_16x16x32_bf16 v[232:235], a[28:31], v[228:231], v[232:235]\n"
    "v_mfma_f32_16x16x32_bf16 v[236:239], a[28:31], v[148:151], v[236:239]\n"
    "v_mfma_f32_16x16x32_bf16 v[104:107], a[28:31], v[128:131], v[104:107]\n"
    "v_mfma_f32_16x16x32_bf16 v[8:11], a[28:31], v[136:139], v[8:11]\n"
    "v_mfma_f32_16x16x32_bf16 v[120:123], a[52:55], v[228:231], v[120:123]\n"
    "v_mfma_f32_16x16x32_bf16 v[28:31], a[52:55], v[148:151], v[28:31]\n"
    "v_mfma_f32_16x16x32_bf16 v[72:75], a[52:55], v[128:131], v[72:75]\n"
    "v_mfma_f32_16x16x32_bf16 v[76:79], a[52:55], v[136:139], v[76:79]\n"
    "v_mfma_f32_16x16x32_bf16 v[68:71], a[76:79], v[228:231], v[68:71]\n"
    "v_mfma_f32_16x16x32_bf16 v[108:111], a[76:79], v[148:151], v[108:111]\n"
    "buffer_load_dwordx4 v244, s[48:51], s100 offen offset:1024 lds\n"
    "v_mfma_f32_16x16x32_bf16 v[88:91], a[76:79], v[128:131], v[88:91]\n"
    "ds_read_b128 v[240:243], v205 offset:8192\n"
    "v_mfma_f32_16x16x32_bf16 v[92:95], a[76:79], v[136:139], v[92:95]\n"
    "ds_read_b128 v[12:15], v205 offset:10240\n"
    "v_mfma_f32_16x16x32_bf16 v[96:99], a[100:103], v[228:231], v[96:99]\n"
    "ds_read_b128 v[16:19], v205 offset:12288\n"
    "v_mfma_f32_16x16x32_bf16 v[100:103], a[100:103], v[148:151], v[100:103]\n"
    "ds_read_b128 v[20:23], v205 offset:14336\n"
    "v_mfma_f32_16x16x32_bf16 v[224:227], a[100:103], v[128:131], v[224:227]\n"
    "v_mfma_f32_16x16x32_bf16 v[112:115], a[100:103], v[136:139], v[112:115]\n"
    "v_mfma_f32_16x16x32_bf16 v[116:119], a[124:127], v[228:231], v[116:119]\n"
    "v_mfma_f32_16x16x32_bf16 v[132:135], a[124:127], v[148:151], v[132:135]\n"
    "v_mfma_f32_16x16x32_bf16 v[0:3], a[124:127], v[128:131], v[0:3]\n"
    "v_mfma_f32_16x16x32_bf16 v[124:127], a[124:127], v[136:139], v[124:127]\n"
    "buffer_load_dwordx4 v245, s[48:51], s100 offen offset:2048 lds\n"
    "s_mov_b32 m0, s74\n"
    "s_add_u32 s100, s35, 110592\n"
    "s_waitcnt vmcnt(6)\n"
    "ds_read_b64_tr_b16 a[8:9], v201 offset:0\n"
    "ds_read_b64_tr_b16 a[10:11], v201 offset:768\n"
    "ds_read_b64_tr_b16 a[32:33], v201 offset:32\n"
    "ds_read_b64_tr_b16 a[34:35], v201 offset:800\n"
    "ds_read_b64_tr_b16 a[56:57], v201 offset:64\n"
    "ds_read_b64_tr_b16 a[58:59], v201 offset:832\n"
    "s_waitcnt lgkmcnt(4)\n"
    "v_mfma_f32_16x16x32_bf16 v[180:183], a[8:11], v[240:243], v[180:183]\n"
    "v_mfma_f32_16x16x32_bf16 v[144:147], a[8:11], v[12:15], v[144:147]\n"
    "v_mfma_f32_16x16x32_bf16 v[196:199], a[8:11], v[16:19], v[196:199]\n"
    "s_waitcnt lgkmcnt(0)\n"
    "buffer_load_dwordx4 v248, s[48:51], s100 offen offset:0 lds\n"
    "v_mfma_f32_16x16x32_bf16 v[4:7], a[8:11], v[20:23], v[4:7]\n"
    "buffer_load_dwordx4 v244, s[48:51], s100 offen offset:1024 lds\n"
    "v_mfma_f32_16x16x32_bf16 v[232:235], a[32:35], v[240:243], v[232:235]\n"
    "buffer_load_dwordx4 v245, s[48:51], s100 offen offset:2048 lds\n"
    "v_mfma_f32_16x16x32_bf16 v[236:239], a[32:35], v[12:15], v[236:239]\n"
    "s_waitcnt vmcnt(6)\n"
    "ds_read_b64_tr_b16 a[80:81], v201 offset:3072\n"
    "ds_read_b64_tr_b16 a[82:83], v201 offset:3840\n"
    "v_mfma_f32_16x16x32_bf16 v[104:107], a[32:35], v[16:19], v[104:107]\n"
    "ds_read_b64_tr_b16 a[104:105], v201 offset:3104\n"
    "s_add_u32 m0, s74, 3072\n"
    "v_mfma_f32_16x16x32_bf16 v[8:11], a[32:35], v[20:23], v[8:11]\n"
    "ds_read_b64_tr_b16 a[106:107], v201 offset:3872\n"
    "s_add_u32 s100, s35, 147840\n"
    "v_mfma_f32_16x16x32_bf16 v[120:123], a[56:59], v[240:243], v[120:123]\n"
    "ds_read_b64_tr_b16 a[128:129], v201 offset:3136\n"
    "v_mfma_f32_16x16x32_bf16 v[28:31], a[56:59], v[12:15], v[28:31]\n"
    "ds_read_b64_tr_b16 a[130:131], v201 offset:3904\n"
    "v_mfma_f32_16x16x32_bf16 v[72:75], a[56:59], v[16:19], v[72:75]\n"
    "v_mfma_f32_16x16x32_bf16 v[76:79], a[56:59], v[20:23], v[76:79]\n"
    "s_waitcnt lgkmcnt(4)\n"
    "v_mfma_f32_16x16x32_bf16 v[68:71], a[80:83], v[240:243], v[68:71]\n"
    "v_mfma_f32_16x16x32_bf16 v[108:111], a[80:83], v[12:15], v[108:111]\n"
    "ds_read_b128 v[228:231], v206 offset:8192\n"
    "v_mfma_f32_16x16x32_bf16 v[88:91], a[80:83], v[16:19], v[88:91]\n"
    "ds_read_b128 v[80:83], v206 offset:10240\n"
    "v_mfma_f32_16x16x32_bf16 v[92:95], a[80:83], v[20:23], v[92:95]\n"
    "ds_read_b128 v[24:27], v206 offset:12288\n"
    "s_waitcnt lgkmcnt(5)\n"
    "v_mfma_f32_16x16x32_bf16 v[96:99], a[104:107], v[240:243], v[96:99]\n"
    "s_waitcnt lgkmcnt(3)\n"
    "buffer_load_dwordx4 v248, s[48:51], s100 offen offset:0 lds\n"
    "v_mfma_f32_16x16x32_bf16 v[100:103], a[104:107], v[12:15], v[100:103]\n"
    "buffer_load_dwordx4 v244, s[48:51], s100 offen offset:1024 lds\n"
    "v_mfma_f32_16x16x32_bf16 v[224:227], a[104:107], v[16:19], v[224:227]\n"
    "buffer_load_dwordx4 v245, s[48:51], s100 offen offset:2048 lds\n"
    "v_mfma_f32_16x16x32_bf16 v[112:115], a[104:107], v[20:23], v[112:115]\n"
    "s_waitcnt vmcnt(6)\n"
    "ds_read_b64_tr_b16 a[12:13], v201 offset:6144\n"
    "s_add_u32 m0, s74, 6144\n"
    "v_mfma_f32_16x16x32_bf16 v[116:119], a[128:131], v[240:243], v[116:119]\n"
    "ds_read_b64_tr_b16 a[14:15], v201 offset:6912\n"
    "s_add_u32 s100, s35, 147456\n"
    "v_mfma_f32_16x16x32_bf16 v[132:135], a[128:131], v[12:15], v[132:135]\n"
    "ds_read_b64_tr_b16 a[36:37], v201 offset:6176\n"
    "v_mfma_f32_16x16x32_bf16 v[0:3], a[128:131], v[16:19], v[0:3]\n"
    "ds_read_b64_tr_b16 a[38:39], v201 offset:6944\n"
    "v_mfma_f32_16x16x32_bf16 v[124:127], a[128:131], v[20:23], v[124:127]\n"
    "ds_read_b64_tr_b16 a[60:61], v201 offset:6208\n"
    "ds_read_b64_tr_b16 a[62:63], v201 offset:6976\n"
    "ds_read_b128 v[48:51], v206 offset:14336\n"
    "s_waitcnt lgkmcnt(5)\n"
    "v_mfma_f32_16x16x32_bf16 v[180:183], a[12:15], v[228:231], v[180:183]\n"
    "v_mfma_f32_16x16x32_bf16 v[144:147], a[12:15], v[80:83], v[144:147]\n"
    "v_mfma_f32_16x16x32_bf16 v[196:199], a[12:15], v[24:27], v[196:199]\n"
    "s_waitcnt lgkmcnt(0)\n"
    "v_mfma_f32_16x16x32_bf16 v[4:7], a[12:15], v[48:51], v[4:7]\n"
    "buffer_load_dwordx4 v248, s[48:51], s100 offen offset:0 lds\n"
    "v_mfma_f32_16x16x32_bf16 v[232:235], a[36:39], v[228:231], v[232:235]\n"
    "buffer_load_dwordx4 v244, s[48:51], s100 offen offset:1024 lds\n"
    "v_mfma_f32_16x16x32_bf16 v[236:239], a[36:39], v[80:83], v[236:239]\n"
    "buffer_load_dwordx4 v245, s[48:51], s100 offen offset:2048 lds\n"
    "v_mfma_f32_16x16x32_bf16 v[104:107], a[36:39], v[24:27], v[104:107]\n"
    "s_mov_b32 m0, s74\n"
    "s_add_u32 s100, s35, 184704\n"
    "v_mfma_f32_16x16x32_bf16 v[8:11], a[36:39], v[48:51], v[8:11]\n"
    "v_mfma_f32_16x16x32_bf16 v[120:123], a[60:63], v[228:231], v[120:123]\n"
    "v_mfma_f32_16x16x32_bf16 v[28:31], a[60:63], v[80:83], v[28:31]\n"
    "v_mfma_f32_16x16x32_bf16 v[72:75], a[60:63], v[24:27], v[72:75]\n"
    "v_mfma_f32_16x16x32_bf16 v[76:79], a[60:63], v[48:51], v[76:79]\n"
    "s_waitcnt vmcnt(6)\n"
    "ds_read_b64_tr_b16 a[84:85], v201 offset:0\n"
    "ds_read_b64_tr_b16 a[86:87], v201 offset:768\n"
    "ds_read_b64_tr_b16 a[108:109], v201 offset:32\n"
    "ds_read_b64_tr_b16 a[110:111], v201 offset:800\n"
    "ds_read_b64_tr_b16 a[132:133], v201 offset:64\n"
    "ds_read_b64_tr_b16 a[134:135], v201 offset:832\n"
    "s_waitcnt lgkmcnt(4)\n"
    "v_mfma_f32_16x16x32_bf16 v[68:71], a[84:87], v[228:231], v[68:71]\n"
    "v_mfma_f32_16x16x32_bf16 v[108:111], a[84:87], v[80:83], v[108:111]\n"
    "v_mfma_f32_16x16x32_bf16 v[88:91], a[84:87], v[24:27], v[88:91]\n"
    "s_waitcnt lgkmcnt(0)\n"
    "buffer_load_dwordx4 v248, s[48:51], s100 offen offset:0 lds\n"
    "v_mfma_f32_16x16x32_bf16 v[92:95], a[84:87], v[48:51], v[92:95]\n"
    "buffer_load_dwordx4 v244, s[48:51], s100 offen offset:1024 lds\n"
    "v_mfma_f32_16x16x32_bf16 v[96:99], a[108:111], v[228:231], v[96:99]\n"
    "buffer_load_dwordx4 v245, s[48:51], s100 offen offset:2048 lds\n"
    "v_mfma_f32_16x16x32_bf16 v[100:103], a[108:111], v[80:83], v[100:103]\n"
    "s_add_u32 m0, s74, 3072\n"
    "s_add_u32 s100, s35, 184320\n"
    "v_mfma_f32_16x16x32_bf16 v[224:227], a[108:111], v[24:27], v[224:227]\n"
    "v_mfma_f32_16x16x32_bf16 v[112:115], a[108:111], v[48:51], v[112:115]\n"
    "s_waitcnt vmcnt(6)\n"
    "ds_read_b64_tr_b16 a[16:17], v201 offset:3072\n"
    "v_mfma_f32_16x16x32_bf16 v[116:119], a[132:135], v[228:231], v[116:119]\n"
    "ds_read_b64_tr_b16 a[18:19], v201 offset:3840\n"
    "v_mfma_f32_16x16x32_bf16 v[132:135], a[132:135], v[80:83], v[132:135]\n"
    "ds_read_b64_tr_b16 a[40:41], v201 offset:3104\n"
    "v_mfma_f32_16x16x32_bf16 v[0:3], a[132:135], v[24:27], v[0:3]\n"
    "ds_read_b64_tr_b16 a[42:43], v201 offset:3872\n"
    "v_mfma_f32_16x16x32_bf16 v[124:127], a[132:135], v[48:51], v[124:127]\n"
    "s_barrier\n"
    "ds_read_b64_tr_b16 a[64:65], v201 offset:3136\n"
    "ds_read_b64_tr_b16 a[66:67], v201 offset:3904\n"
    "ds_read_b128 v[184:187], v205 offset:16384\n"
    "ds_read_b128 v[12:15], v205 offset:18432\n"
    "ds_read_b128 v[128:131], v205 offset:20480\n"
    "ds_read_b128 v[40:43], v205 offset:22528\n"
    "s_waitcnt lgkmcnt(3)\n"
    "v_mfma_f32_16x16x32_bf16 v[180:183], a[16:19], v[184:187], v[180:183]\n"
    "buffer_load_dwordx4 v248, s[48:51], s100 offen offset:0 lds\n"
    "s_waitcnt lgkmcnt(2)\n"
    "v_mfma_f32_16x16x32_bf16 v[144:147], a[16:19], v[12:15], v[144:147]\n"
    "buffer_load_dwordx4 v244, s[48:51], s100 offen offset:1024 lds\n"
    "s_waitcnt lgkmcnt(1)\n"
    "v_mfma_f32_16x16x32_bf16 v[196:199], a[16:19], v[128:131], v[196:199]\n"
    "buffer_load_dwordx4 v245, s[48:51], s100 offen offset:2048 lds\n"
    "s_waitcnt lgkmcnt(0)\n"
    "v_mfma_f32_16x16x32_bf16 v[4:7], a[16:19], v[40:43], v[4:7]\n"
    "s_waitcnt vmcnt(6)\n"
    "ds_read_b64_tr_b16 a[88:89], v201 offset:6144\n"
    "s_add_u32 m0, s74, 6144\n"
    "v_mfma_f32_16x16x32_bf16 v[232:235], a[40:43], v[184:187], v[232:235]\n"
    "ds_read_b64_tr_b16 a[90:91], v201 offset:6912\n"
    "s_add_u32 s100, s35, 768\n"
    "v_mfma_f32_16x16x32_bf16 v[236:239], a[40:43], v[12:15], v[236:239]\n"
    "ds_read_b64_tr_b16 a[112:113], v201 offset:6176\n"
    "v_mfma_f32_16x16x32_bf16 v[104:107], a[40:43], v[128:131], v[104:107]\n"
    "ds_read_b64_tr_b16 a[114:115], v201 offset:6944\n"
    "v_mfma_f32_16x16x32_bf16 v[8:11], a[40:43], v[40:43], v[8:11]\n"
    "ds_read_b64_tr_b16 a[136:137], v201 offset:6208\n"
    "v_mfma_f32_16x16x32_bf16 v[120:123], a[64:67], v[184:187], v[120:123]\n"
    "ds_read_b64_tr_b16 a[138:139], v201 offset:6976\n"
    "v_mfma_f32_16x16x32_bf16 v[28:31], a[64:67], v[12:15], v[28:31]\n"
    "v_mfma_f32_16x16x32_bf16 v[72:75], a[64:67], v[128:131], v[72:75]\n"
    "v_mfma_f32_16x16x32_bf16 v[76:79], a[64:67], v[40:43], v[76:79]\n"
    "s_waitcnt lgkmcnt(4)\n"
    "v_mfma_f32_16x16x32_bf16 v[68:71], a[88:91], v[184:187], v[68:71]\n"
    "v_mfma_f32_16x16x32_bf16 v[108:111], a[88:91], v[12:15], v[108:111]\n"
    "v_mfma_f32_16x16x32_bf16 v[88:91], a[88:91], v[128:131], v[88:91]\n"
    "ds_read_b128 v[56:59], v206 offset:16384\n"
    "v_mfma_f32_16x16x32_bf16 v[92:95], a[88:91], v[40:43], v[92:95]\n"
    "ds_read_b128 v[228:231], v206 offset:18432\n"
    "s_waitcnt lgkmcnt(4)\n"
    "v_mfma_f32_16x16x32_bf16 v[96:99], a[112:115], v[184:187], v[96:99]\n"
    "s_waitcnt lgkmcnt(2)\n"
    "buffer_load_dwordx4 v248, s[48:51], s100 offen offset:0 lds\n"
    "v_mfma_f32_16x16x32_bf16 v[100:103], a[112:115], v[12:15], v[100:103]\n"
    "buffer_load_dwordx4 v244, s[48:51], s100 offen offset:1024 lds\n"
    "v_mfma_f32_16x16x32_bf16 v[224:227], a[112:115], v[128:131], v[224:227]\n"
    "buffer_load_dwordx4 v245, s[48:51], s100 offen offset:2048 lds\n"
    "v_mfma_f32_16x16x32_bf16 v[112:115], a[112:115], v[40:43], v[112:115]\n"
    "s_add_u32 m0, s72, 8192\n"
    "s_add_u32 s100, s35, 37632\n"
    "v_mfma_f32_16x16x32_bf16 v[116:119], a[136:139], v[184:187], v[116:119]\n"
    "buffer_load_dwordx4 v216, s[36:39], s101 offen offset:0 nt lds\n"
    "v_mfma_f32_16x16x32_bf16 v[132:135], a[136:139], v[12:15], v[132:135]\n"
    "s_mov_b32 m0, s74\n"
    "s_add_u32 s101, s93, 128\n"
    "v_mfma_f32_16x16x32_bf16 v[0:3], a[136:139], v[128:131], v[0:3]\n"
    "ds_read_b128 v[240:243], v206 offset:20480\n"
    "v_mfma_f32_16x16x32_bf16 v[124:127], a[136:139], v[40:43], v[124:127]\n"
    "s_barrier\n"
    "s_waitcnt vmcnt(7)\n"
    "ds_read_b64_tr_b16 a[20:21], v201 offset:0\n"
    "ds_read_b64_tr_b16 a[22:23], v201 offset:768\n"
    "ds_read_b64_tr_b16 a[44:45], v201 offset:32\n"
    "ds_read_b64_tr_b16 a[46:47], v201 offset:800\n"
    "ds_read_b64_tr_b16 a[68:69], v201 offset:64\n"
    "ds_read_b64_tr_b16 a[70:71], v201 offset:832\n"
    "ds_read_b128 v[12:15], v206 offset:22528\n"
    "s_waitcnt lgkmcnt(5)\n"
    "v_mfma_f32_16x16x32_bf16 v[180:183], a[20:23], v[56:59], v[180:183]\n"
    "v_mfma_f32_16x16x32_bf16 v[144:147], a[20:23], v[228:231], v[144:147]\n"
    "v_mfma_f32_16x16x32_bf16 v[196:199], a[20:23], v[240:243], v[196:199]\n"
    "s_waitcnt lgkmcnt(1)\n"
    "buffer_load_dwordx4 v248, s[48:51], s100 offen offset:0 lds\n"
    "s_waitcnt lgkmcnt(0)\n"
    "v_mfma_f32_16x16x32_bf16 v[4:7], a[20:23], v[12:15], v[4:7]\n"
    "buffer_load_dwordx4 v244, s[48:51], s100 offen offset:1024 lds\n"
    "v_mfma_f32_16x16x32_bf16 v[232:235], a[44:47], v[56:59], v[232:235]\n"
    "buffer_load_dwordx4 v245, s[48:51], s100 offen offset:2048 lds\n"
    "v_mfma_f32_16x16x32_bf16 v[236:239], a[44:47], v[228:231], v[236:239]\n"
    "s_add_u32 m0, s72, 8192\n"
    "s_add_u32 s100, s35, 74496\n"
    "v_cvt_pk_bf16_f32 v36, v180, v181\n"
    "v_mfma_f32_16x16x32_bf16 v[104:107], a[44:47], v[240:243], v[104:107]\n"
    "buffer_load_dwordx4 v217, s[36:39], s101 offen offset:1024 nt lds\n"
    "v_mfma_f32_16x16x32_bf16 v[8:11], a[44:47], v[12:15], v[8:11]\n"
    "s_add_u32 m0, s74, 3072\n"
    "s_add_u32 s101, s93, 256\n"
    "v_cvt_pk_bf16_f32 v37, v182, v183\n"
    "v_mfma_f32_16x16x32_bf16 v[120:123], a[68:71], v[56:59], v[120:123]\n"
    "ds_read_b128 v[136:139], v209 offset:0\n"
    "v_cvt_pk_bf16_f32 v40, v144, v145\n"
    "v_mfma_f32_16x16x32_bf16 v[28:31], a[68:71], v[228:231], v[28:31]\n"
    "ds_read_b128 v[140:143], v210 offset:1024\n"
    "v_cvt_pk_bf16_f32 v41, v146, v147\n"
    "v_mfma_f32_16x16x32_bf16 v[72:75], a[68:71], v[240:243], v[72:75]\n"
    "ds_read_b128 v[144:147], v209 offset:2048\n"
    "v_cvt_pk_bf16_f32 v44, v196, v197\n"
    "v_mfma_f32_16x16x32_bf16 v[76:79], a[68:71], v[12:15], v[76:79]\n"
    "s_waitcnt vmcnt(8)\n"
    "ds_read_b64_tr_b16 a[92:93], v201 offset:3072\n"
    "ds_read_b64_tr_b16 a[94:95], v201 offset:3840\n"
    "ds_read_b64_tr_b16 a[116:117], v201 offset:3104\n"
    "ds_read_b64_tr_b16 a[118:119], v201 offset:3872\n"
    "ds_read_b64_tr_b16 a[140:141], v201 offset:3136\n"
    "ds_read_b64_tr_b16 a[142:143], v201 offset:3904\n"
    "ds_read_b128 v[148:151], v210 offset:3072\n"
    "ds_read_b128 v[170:173], v205 offset:24576\n"
    "ds_read_b128 v[176:179], v206 offset:24576\n"
    "ds_read_b128 v[152:155], v209 offset:4096\n"
    "ds_read_b128 v[156:159], v210 offset:5120\n"
    "ds_read_b128 v[160:163], v209 offset:6144\n"
    "ds_read_b128 v[164:167], v210 offset:7168\n"
    "ds_read_b128 v[184:187], v205 offset:26624\n"
    "ds_read_b128 v[190:193], v206 offset:26624\n"
    "v_cvt_pk_bf16_f32 v45, v198, v199\n"
    "s_waitcnt lgkmcnt(13)\n"
    "v_mfma_f32_16x16x32_bf16 v[68:71], a[92:95], v[56:59], v[68:71]\n"
    "s_waitcnt lgkmcnt(11)\n"
    "v_cvt_pk_bf16_f32 v48, v4, v5\n"
    "v_cvt_pk_bf16_f32 v49, v6, v7\n"
    "v_mfma_f32_16x16x32_bf16 v[96:99], a[116:119], v[56:59], v[96:99]\n"
    "s_waitcnt lgkmcnt(9)\n"
    "v_cvt_pk_bf16_f32 v38, v232, v233\n"
    "v_cvt_pk_bf16_f32 v39, v234, v235\n"
    "v_mfma_f32_16x16x32_bf16 v[116:119], a[140:143], v[56:59], v[116:119]\n"
    "buffer_load_dwordx4 v248, s[48:51], s100 offen offset:0 lds\n"
    "v_mfma_f32_16x16x32_bf16 v[108:111], a[92:95], v[228:231], v[108:111]\n"
    "buffer_load_dwordx4 v244, s[48:51], s100 offen offset:1024 lds\n"
    "v_mfma_f32_16x16x32_bf16 v[100:103], a[116:119], v[228:231], v[100:103]\n"
    "buffer_load_dwordx4 v245, s[48:51], s100 offen offset:2048 lds\n"
    "v_mfma_f32_16x16x32_bf16 v[132:135], a[140:143], v[228:231], v[132:135]\n"
    "s_add_u32 m0, s72, 16384\n"
    "s_add_u32 s100, s35, 111360\n"
    "v_cvt_pk_bf16_f32 v42, v236, v237\n"
    "v_mfma_f32_16x16x32_bf16 v[88:91], a[92:95], v[240:243], v[88:91]\n"
    "buffer_load_dwordx4 v216, s[36:39], s101 offen offset:0 nt lds\n"
    "v_mfma_f32_16x16x32_bf16 v[224:227], a[116:119], v[240:243], v[224:227]\n"
    "s_add_u32 m0, s74, 6144\n"
    "s_add_u32 s101, s93, 256\n"
    "v_cvt_pk_bf16_f32 v43, v238, v239\n"
    "v_mfma_f32_16x16x32_bf16 v[0:3], a[140:143], v[240:243], v[0:3]\n"
    "v_cvt_pk_bf16_f32 v46, v104, v105\n"
    "v_cvt_pk_bf16_f32 v47, v106, v107\n"
    "v_mfma_f32_16x16x32_bf16 v[92:95], a[92:95], v[12:15], v[92:95]\n"
    "v_cvt_pk_bf16_f32 v50, v8, v9\n"
    "v_cvt_pk_bf16_f32 v51, v10, v11\n"
    "v_mfma_f32_16x16x32_bf16 v[112:115], a[116:119], v[12:15], v[112:115]\n"
    "v_cvt_pk_bf16_f32 v52, v120, v121\n"
    "v_cvt_pk_bf16_f32 v53, v122, v123\n"
    "v_mfma_f32_16x16x32_bf16 v[124:127], a[140:143], v[12:15], v[124:127]\n"
    "v_cvt_pk_bf16_f32 v56, v28, v29\n"
    "v_cvt_pk_bf16_f32 v57, v30, v31\n"
    "ds_read_b128 v[104:107], v209 offset:8192\n"
    "ds_read_b128 v[120:123], v210 offset:9216\n"
    "ds_read_b128 v[128:131], v209 offset:10240\n"
    "ds_read_b128 v[196:199], v210 offset:11264\n"
    "ds_read_b128 v[4:7], v205 offset:28672\n"
    "ds_read_b128 v[10:13], v206 offset:28672\n"
    "v_cvt_pk_bf16_f32 v58, v72, v73\n"
    "v_cvt_pk_bf16_f32 v59, v74, v75\n"
    "v_cvt_pk_bf16_f32 v62, v76, v77\n"
    "v_cvt_pk_bf16_f32 v63, v78, v79\n"
    "v_cvt_pk_bf16_f32 v64, v68, v69\n"
    "v_cvt_pk_bf16_f32 v65, v70, v71\n"
    "v_cvt_pk_bf16_f32 v66, v96, v97\n"
    "v_cvt_pk_bf16_f32 v67, v98, v99\n"
    "v_cvt_pk_bf16_f32 v80, v116, v117\n"
    "v_cvt_pk_bf16_f32 v81, v118, v119\n"
    "v_mfma_f32_16x16x32_bf16 v[136:139], v[36:39], v[64:67], v[136:139]\n"
    "v_cvt_pk_bf16_f32 v68, v108, v109\n"
    "v_cvt_pk_bf16_f32 v69, v110, v111\n"
    "v_cvt_pk_bf16_f32 v70, v100, v101\n"
    "v_cvt_pk_bf16_f32 v71, v102, v103\n"
    "v_cvt_pk_bf16_f32 v82, v132, v133\n"
    "v_cvt_pk_bf16_f32 v83, v134, v135\n"
    "v_cvt_pk_bf16_f32 v72, v88, v89\n"
    "v_cvt_pk_bf16_f32 v73, v90, v91\n"
    "v_cvt_pk_bf16_f32 v74, v224, v225\n"
    "v_cvt_pk_bf16_f32 v75, v226, v227\n"
    "v_cvt_pk_bf16_f32 v84, v0, v1\n"
    "v_cvt_pk_bf16_f32 v85, v2, v3\n"
    "v_cvt_pk_bf16_f32 v76, v92, v93\n"
    "v_cvt_pk_bf16_f32 v77, v94, v95\n"
    "v_cvt_pk_bf16_f32 v78, v112, v113\n"
    "v_cvt_pk_bf16_f32 v79, v114, v115\n"
    "v_cvt_pk_bf16_f32 v86, v124, v125\n"
    "v_cvt_pk_bf16_f32 v87, v126, v127\n"
    "v_mov_b64 v[168:169], v[80:81]\n"
    "v_mov_b64 v[174:175], v[80:81]\n"
    "v_mov_b64 v[180:181], v[80:81]\n"
    "s_waitcnt lgkmcnt(5)\n"
    "v_mfma_f32_16x16x32_bf16 v[136:139], v[52:55], v[168:171], v[136:139]\n"
    "s_waitcnt vmcnt(9)\n"
    "ds_read_b64_tr_b16 a[144:145], v201 offset:6144\n"
    "v_mov_b64 v[182:183], v[82:83]\n"
    "v_mfma_f32_16x16x32_bf16 v[140:143], v[40:43], v[64:67], v[140:143]\n"
    "ds_read_b64_tr_b16 a[146:147], v201 offset:6912\n"
    "v_mov_b64 v[188:189], v[82:83]\n"
    "v_mfma_f32_16x16x32_bf16 v[140:143], v[54:57], v[172:175], v[140:143]\n"
    "ds_read_b64_tr_b16 a[168:169], v201 offset:6176\n"
    "v_mov_b64 v[194:195], v[82:83]\n"
    "v_mfma_f32_16x16x32_bf16 v[144:147], v[44:47], v[64:67], v[144:147]\n"
    "ds_read_b64_tr_b16 a[170:171], v201 offset:6944\n"
    "v_mov_b64 v[2:3], v[84:85]\n"
    "v_mfma_f32_16x16x32_bf16 v[144:147], v[58:61], v[174:177], v[144:147]\n"
    "ds_read_b64_tr_b16 a[192:193], v201 offset:6208\n"
    "v_mov_b64 v[8:9], v[84:85]\n"
    "v_mfma_f32_16x16x32_bf16 v[148:151], v[48:51], v[64:67], v[148:151]\n"
    "ds_read_b64_tr_b16 a[194:195], v201 offset:6976\n"
    "v_mov_b64 v[14:15], v[84:85]\n"
    "v_mfma_f32_16x16x32_bf16 v[148:151], v[60:63], v[178:181], v[148:151]\n"
    "ds_read_b128 v[16:19], v209 offset:12288\n"
    "v_mov_b64 v[20:21], v[86:87]\n"
    "v_mfma_f32_16x16x32_bf16 v[152:155], v[36:39], v[68:71], v[152:155]\n"
    "ds_read_b128 v[116:119], v210 offset:13312\n"
    "v_mov_b64 v[26:27], v[86:87]\n"
    "v_mfma_f32_16x16x32_bf16 v[152:155], v[52:55], v[182:185], v[152:155]\n"
    "ds_read_b128 v[124:127], v209 offset:14336\n"
    "v_mov_b64 v[32:33], v[86:87]\n"
    "v_mfma_f32_16x16x32_bf16 v[156:159], v[40:43], v[68:71], v[156:159]\n"
    "ds_read_b128 v[84:87], v210 offset:15360\n"
    "v_max3_f32 v253, v136, v137, v138\n"
    "v_mfma_f32_16x16x32_bf16 v[156:159], v[54:57], v[186:189], v[156:159]\n"
    "ds_read_b128 v[22:25], v205 offset:30720\n"
    "v_max3_f32 v252, v139, v140, v141\n"
    "v_mfma_f32_16x16x32_bf16 v[160:163], v[44:47], v[68:71], v[160:163]\n"
    "ds_read_b128 v[28:31], v206 offset:30720\n"
    "v_max3_f32 v251, v152, v153, v154\n"
    "v_mfma_f32_16x16x32_bf16 v[160:163], v[58:61], v[188:191], v[160:163]\n"
    "v_max3_f32 v253, v253, v142, v143\n"
    "v_max3_f32 v252, v252, v144, v145\n"
    "v_mfma_f32_16x16x32_bf16 v[164:167], v[48:51], v[68:71], v[164:167]\n"
    "s_waitcnt lgkmcnt(6)\n"
    "buffer_load_dwordx4 v248, s[48:51], s100 offen offset:0 lds\n"
    "v_mfma_f32_16x16x32_bf16 v[164:167], v[60:63], v[192:195], v[164:167]\n"
    "v_max3_f32 v250, v155, v156, v157\n"
    "v_max3_f32 v251, v251, v158, v159\n"
    "v_mfma_f32_16x16x32_bf16 v[104:107], v[36:39], v[72:75], v[104:107]\n"
    "v_max3_f32 v253, v253, v146, v147\n"
    "v_max3_f32 v250, v250, v160, v161\n"
    "v_mfma_f32_16x16x32_bf16 v[104:107], v[52:55], v[2:5], v[104:107]\n"
    "v_max3_f32 v251, v251, v162, v163\n"
    "v_max3_f32 v252, v252, v148, v149\n"
    "v_mfma_f32_16x16x32_bf16 v[120:123], v[40:43], v[72:75], v[120:123]\n"
    "v_max3_f32 v253, v253, v150, v151\n"
    "v_max3_f32 v250, v250, v164, v165\n"
    "v_mfma_f32_16x16x32_bf16 v[120:123], v[54:57], v[6:9], v[120:123]\n"
    "v_max3_f32 v251, v251, v166, v167\n"
    "v_max_f32 v253, v253, v252\n"
    "v_mfma_f32_16x16x32_bf16 v[128:131], v[44:47], v[72:75], v[128:131]\n"
    "v_max_f32 v251, v251, v250\n"
    "v_max3_f32 v252, v104, v105, v106\n"
    "v_mfma_f32_16x16x32_bf16 v[128:131], v[58:61], v[8:11], v[128:131]\n"
    "ds_read_b128 v[224:227], v205 offset:0\n"
    "v_mfma_f32_16x16x32_bf16 v[196:199], v[48:51], v[72:75], v[196:199]\n"
    "ds_read_b128 v[240:243], v205 offset:2048\n"
    "v_max3_f32 v250, v107, v120, v121\n"
    "v_mfma_f32_16x16x32_bf16 v[196:199], v[60:63], v[12:15], v[196:199]\n"
    "ds_read_b128 v[168:171], v205 offset:4096\n"
    "v_max3_f32 v252, v252, v122, v123\n"
    "s_waitcnt lgkmcnt(4)\n"
    "v_mfma_f32_16x16x32_bf16 v[16:19], v[36:39], v[76:79], v[16:19]\n"
    "ds_read_b128 v[64:67], v205 offset:6144\n"
    "v_max3_f32 v250, v250, v128, v129\n"
    "v_mfma_f32_16x16x32_bf16 v[16:19], v[52:55], v[20:23], v[16:19]\n"
    "v_permlane32_swap_b32 v253, v251\n"
    "v_mfma_f32_16x16x32_bf16 v[116:119], v[40:43], v[76:79], v[116:119]\n"
    "v_max_f32 v253, v253, v251\n"
    "v_max3_f32 v252, v252, v130, v131\n"
    "v_mfma_f32_16x16x32_bf16 v[116:119], v[54:57], v[24:27], v[116:119]\n"
    "v_mov_b32 v251, v253\n"
    "v_max3_f32 v250, v250, v196, v197\n"
    "v_mfma_f32_16x16x32_bf16 v[124:127], v[44:47], v[76:79], v[124:127]\n"
    "s_waitcnt lgkmcnt(4)\n"
    "v_permlane16_swap_b32 v253, v251\n"
    "v_mfma_f32_16x16x32_bf16 v[124:127], v[58:61], v[26:29], v[124:127]\n"
    "v_max_f32 v253, v253, v251\n"
    "v_max3_f32 v204, v16, v17, v18\n"
    "v_mfma_f32_16x16x32_bf16 v[84:87], v[48:51], v[76:79], v[84:87]\n"
    "v_mul_f32 v253, s71, v253\n"
    "v_max3_f32 v249, v19, v116, v117\n"
    "v_mfma_f32_16x16x32_bf16 v[84:87], v[60:63], v[30:33], v[84:87]\n"
    "s_waitcnt lgkmcnt(3)\n"
    "v_mov_b32 v251, v253\n"
    "v_max3_f32 v204, v204, v118, v119\n"
    "v_mfma_f32_16x16x32_bf16 v[108:111], v[224:227], a[144:147], 0\n"
    "s_waitcnt lgkmcnt(2)\n"
    "v_permlane32_swap_b32 v253, v251\n"
    "v_mfma_f32_16x16x32_bf16 v[132:135], v[240:243], a[144:147], 0\n"
    "ds_read_b128 v[180:183], v206 offset:0\n"
    "v_fma_f32 v136, v136, s68, v253\n"
    "s_waitcnt lgkmcnt(2)\n"
    "v_mfma_f32_16x16x32_bf16 v[236:239], v[168:171], a[144:147], 0\n"
    "ds_read_b128 v[88:91], v206 offset:2048\n"
    "v_fma_f32 v137, v137, s68, v253\n"
    "s_waitcnt lgkmcnt(2)\n"
    "v_mfma_f32_16x16x32_bf16 v[100:103], v[64:67], a[144:147], 0\n"
    "buffer_load_dwordx4 v244, s[48:51], s100 offen offset:1024 lds\n"
    "v_mfma_f32_16x16x32_bf16 v[72:75], v[224:227], a[168:171], 0\n"
    "ds_read_b128 v[188:191], v206 offset:4096\n"
    "v_fma_f32 v138, v138, s68, v253\n"
    "v_mfma_f32_16x16x32_bf16 v[68:71], v[240:243], a[168:171], 0\n"
    "ds_read_b128 v[76:79], v206 offset:6144\n"
    "v_fma_f32 v139, v139, s68, v253\n"
    "v_mfma_f32_16x16x32_bf16 v[24:27], v[168:171], a[168:171], 0\n"
    "v_fma_f32 v140, v140, s68, v253\n"
    "v_fma_f32 v141, v141, s68, v253\n"
    "v_mfma_f32_16x16x32_bf16 v[80:83], v[64:67], a[168:171], 0\n"
    "buffer_load_dwordx4 v245, s[48:51], s100 offen offset:2048 lds\n"
    "v_mfma_f32_16x16x32_bf16 v[228:231], v[224:227], a[192:195], 0\n"
    "s_add_u32 m0, s72, 16384\n"
    "s_add_u32 s100, s35, 148224\n"
    "v_fma_f32 v142, v142, s68, v253\n"
    "v_mfma_f32_16x16x32_bf16 v[172:175], v[240:243], a[192:195], 0\n"
    "v_fma_f32 v143, v143, s68, v253\n"
    "v_fma_f32 v144, v144, s68, v253\n"
    "v_mfma_f32_16x16x32_bf16 v[36:39], v[168:171], a[192:195], 0\n"
    "v_exp_f32 v136, v136\n"
    "v_mfma_f32_16x16x32_bf16 v[168:171], v[64:67], a[192:195], 0\n"
    "v_exp_f32 v137, v137\n"
    "buffer_load_dwordx4 v217, s[36:39], s101 offen offset:1024 nt lds\n"
    "s_mov_b32 m0, s74\n"
    "v_exp_f32 v138, v138\n"
    "v_exp_f32 v139, v139\n"
    "v_exp_f32 v140, v140\n"
    "v_exp_f32 v141, v141\n"
    "v_exp_f32 v142, v142\n"
    "v_exp_f32 v143, v143\n"
    "s_waitcnt vmcnt(9)\n"
    "ds_read_b64_tr_b16 a[148:149], v201 offset:0\n"
    "ds_read_b64_tr_b16 a[150:151], v201 offset:768\n"
    "ds_read_b64_tr_b16 a[172:173], v201 offset:32\n"
    "ds_read_b64_tr_b16 a[174:175], v201 offset:800\n"
    "ds_read_b64_tr_b16 a[196:197], v201 offset:64\n"
    "ds_read_b64_tr_b16 a[198:199], v201 offset:832\n"
    "v_fma_f32 v145, v145, s68, v253\n"
    "v_fma_f32 v146, v146, s68, v253\n"
    "v_fma_f32 v147, v147, s68, v253\n"
    "v_fma_f32 v148, v148, s68, v253\n"
    "v_fma_f32 v149, v149, s68, v253\n"
    "v_fma_f32 v150, v150, s68, v253\n"
    "v_fma_f32 v151, v151, s68, v253\n"
    "v_cvt_pk_bf16_f32 v112, v136, v137\n"
    "v_cvt_pk_bf16_f32 v113, v138, v139\n"
    "v_cvt_pk_bf16_f32 v114, v140, v141\n"
    "v_cvt_pk_bf16_f32 v115, v142, v143\n"
    "v_exp_f32 v144, v144\n"
    "v_exp_f32 v145, v145\n"
    "v_mfma_f32_16x16x32_bf16 v[40:43], v[220:223], v[112:115], 0\n"
    "s_waitcnt lgkmcnt(4)\n"
    "v_exp_f32 v146, v146\n"
    "v_mfma_f32_16x16x32_bf16 v[108:111], v[180:183], a[148:151], v[108:111]\n"
    "v_exp_f32 v147, v147\n"
    "v_mfma_f32_16x16x32_bf16 v[132:135], v[88:91], a[148:151], v[132:135]\n"
    "ds_read_b128 v[136:139], v205 offset:8192\n"
    "v_cvt_pk_bf16_f32 v140, v144, v145\n"
    "v_mfma_f32_16x16x32_bf16 v[236:239], v[188:191], a[148:151], v[236:239]\n"
    "s_waitcnt lgkmcnt(1)\n"
    "buffer_load_dwordx4 v248, s[48:51], s100 offen offset:0 lds\n"
    "v_mfma_f32_16x16x32_bf16 v[100:103], v[76:79], a[148:151], v[100:103]\n"
    "buffer_load_dwordx4 v244, s[48:51], s100 offen offset:1024 lds\n"
    "v_mfma_f32_16x16x32_bf16 v[72:75], v[180:183], a[172:175], v[72:75]\n"
    "buffer_load_dwordx4 v245, s[48:51], s100 offen offset:2048 lds\n"
    "v_mfma_f32_16x16x32_bf16 v[68:71], v[88:91], a[172:175], v[68:71]\n"
    "s_add_u32 m0, s74, 3072\n"
    "s_add_u32 s100, s35, 185088\n"
    "v_cvt_pk_bf16_f32 v141, v146, v147\n"
    "v_mfma_f32_16x16x32_bf16 v[24:27], v[188:191], a[172:175], v[24:27]\n"
    "ds_read_b128 v[92:95], v205 offset:10240\n"
    "v_max3_f32 v249, v249, v124, v125\n"
    "v_mfma_f32_16x16x32_bf16 v[80:83], v[76:79], a[172:175], v[80:83]\n"
    "ds_read_b128 v[0:3], v205 offset:12288\n"
    "v_max3_f32 v204, v204, v126, v127\n"
    "v_mfma_f32_16x16x32_bf16 v[228:231], v[180:183], a[196:199], v[228:231]\n"
    "ds_read_b128 v[44:47], v205 offset:14336\n"
    "v_max3_f32 v252, v252, v198, v199\n"
    "v_mfma_f32_16x16x32_bf16 v[172:175], v[88:91], a[196:199], v[172:175]\n"
    "v_exp_f32 v148, v148\n"
    "v_mfma_f32_16x16x32_bf16 v[36:39], v[188:191], a[196:199], v[36:39]\n"
    "v_exp_f32 v149, v149\n"
    "v_mfma_f32_16x16x32_bf16 v[168:171], v[76:79], a[196:199], v[168:171]\n"
    "s_waitcnt vmcnt(8)\n"
    "ds_read_b64_tr_b16 a[152:153], v201 offset:3072\n"
    "ds_read_b64_tr_b16 a[154:155], v201 offset:3840\n"
    "ds_read_b64_tr_b16 a[176:177], v201 offset:3104\n"
    "ds_read_b64_tr_b16 a[178:179], v201 offset:3872\n"
    "ds_read_b64_tr_b16 a[200:201], v201 offset:3136\n"
    "ds_read_b64_tr_b16 a[202:203], v201 offset:3904\n"
    "v_exp_f32 v150, v150\n"
    "v_exp_f32 v151, v151\n"
    "v_cvt_pk_bf16_f32 v142, v148, v149\n"
    "v_cvt_pk_bf16_f32 v143, v150, v151\n"
    "v_max3_f32 v249, v249, v84, v85\n"
    "v_max3_f32 v204, v204, v86, v87\n"
    "v_mfma_f32_16x16x32_bf16 v[40:43], v[220:223], v[140:143], v[40:43]\n"
    "v_max_f32 v252, v252, v250\n"
    "v_max_f32 v204, v204, v249\n"
    "v_fma_f32 v152, v152, s68, v251\n"
    "v_fma_f32 v153, v153, s68, v251\n"
    "v_permlane32_swap_b32 v252, v204\n"
    "v_max_f32 v252, v252, v204\n"
    "v_fma_f32 v154, v154, s68, v251\n"
    "s_waitcnt lgkmcnt(4)\n"
    "v_mfma_f32_16x16x32_bf16 v[108:111], v[136:139], a[152:155], v[108:111]\n"
    "v_rcp_f32 v253, v40\n"
    "v_mfma_f32_16x16x32_bf16 v[132:135], v[92:95], a[152:155], v[132:135]\n"
    "ds_read_b128 v[96:99], v206 offset:8192\n"
    "v_mov_b32 v204, v252\n"
    "v_mfma_f32_16x16x32_bf16 v[236:239], v[0:3], a[152:155], v[236:239]\n"
    "s_waitcnt lgkmcnt(1)\n"
    "buffer_load_dwordx4 v248, s[48:51], s100 offen offset:0 lds\n"
    "v_mfma_f32_16x16x32_bf16 v[100:103], v[44:47], a[152:155], v[100:103]\n"
    "buffer_load_dwordx4 v244, s[48:51], s100 offen offset:1024 lds\n"
    "v_mfma_f32_16x16x32_bf16 v[72:75], v[136:139], a[176:179], v[72:75]\n"
    "buffer_load_dwordx4 v245, s[48:51], s100 offen offset:2048 lds\n"
    "v_mfma_f32_16x16x32_bf16 v[68:71], v[92:95], a[176:179], v[68:71]\n"
    "s_add_u32 m0, s74, 6144\n"
    "s_mov_b32 s100, 0\n"
    "v_fma_f32 v155, v155, s68, v251\n"
    "v_mfma_f32_16x16x32_bf16 v[24:27], v[0:3], a[176:179], v[24:27]\n"
    "ds_read_b128 v[4:7], v206 offset:10240\n"
    "v_fma_f32 v156, v156, s68, v251\n"
    "v_mfma_f32_16x16x32_bf16 v[80:83], v[44:47], a[176:179], v[80:83]\n"
    "ds_read_b128 v[64:67], v206 offset:12288\n"
    "v_fma_f32 v157, v157, s68, v251\n"
    "v_mfma_f32_16x16x32_bf16 v[228:231], v[136:139], a[200:203], v[228:231]\n"
    "ds_read_b128 v[188:191], v206 offset:14336\n"
    "v_fma_f32 v158, v158, s68, v251\n"
    "v_mfma_f32_16x16x32_bf16 v[172:175], v[92:95], a[200:203], v[172:175]\n"
    "v_fma_f32 v159, v159, s68, v251\n"
    "v_fma_f32 v160, v160, s68, v251\n"
    "v_mfma_f32_16x16x32_bf16 v[36:39], v[0:3], a[200:203], v[36:39]\n"
    "v_exp_f32 v152, v152\n"
    "v_mfma_f32_16x16x32_bf16 v[168:171], v[44:47], a[200:203], v[168:171]\n"
    "s_waitcnt vmcnt(7)\n"
    "ds_read_b64_tr_b16 a[156:157], v201 offset:6144\n"
    "ds_read_b64_tr_b16 a[158:159], v201 offset:6912\n"
    "ds_read_b64_tr_b16 a[180:181], v201 offset:6176\n"
    "ds_read_b64_tr_b16 a[182:183], v201 offset:6944\n"
    "ds_read_b64_tr_b16 a[204:205], v201 offset:6208\n"
    "ds_read_b64_tr_b16 a[206:207], v201 offset:6976\n"
    "v_exp_f32 v153, v153\n"
    "v_exp_f32 v154, v154\n"
    "v_exp_f32 v155, v155\n"
    "v_exp_f32 v156, v156\n"
    "v_exp_f32 v157, v157\n"
    "v_exp_f32 v158, v158\n"
    "v_exp_f32 v159, v159\n"
    "v_fma_f32 v161, v161, s68, v251\n"
    "v_fma_f32 v162, v162, s68, v251\n"
    "v_fma_f32 v163, v163, s68, v251\n"
    "v_fma_f32 v164, v164, s68, v251\n"
    "v_fma_f32 v165, v165, s68, v251\n"
    "s_waitcnt lgkmcnt(4)\n"
    "v_mfma_f32_16x16x32_bf16 v[108:111], v[96:99], a[156:159], v[108:111]\n"
    "v_fma_f32 v166, v166, s68, v251\n"
    "v_fma_f32 v167, v167, s68, v251\n"
    "v_mfma_f32_16x16x32_bf16 v[132:135], v[4:7], a[156:159], v[132:135]\n"
    "ds_read_b128 v[232:235], v205 offset:16384\n"
    "v_cvt_pk_bf16_f32 v136, v152, v153\n"
    "v_mfma_f32_16x16x32_bf16 v[236:239], v[64:67], a[156:159], v[236:239]\n"
    "s_waitcnt lgkmcnt(1)\n"
    "buffer_load_dwordx4 v254, s[52:55], s100 offen offset:0 lds\n"
    "v_mfma_f32_16x16x32_bf16 v[100:103], v[188:191], a[156:159], v[100:103]\n"
    "buffer_load_dwordx4 v246, s[52:55], s100 offen offset:1024 lds\n"
    "v_mfma_f32_16x16x32_bf16 v[72:75], v[96:99], a[180:183], v[72:75]\n"
    "buffer_load_dwordx4 v247, s[52:55], s100 offen offset:2048 lds\n"
    "v_mfma_f32_16x16x32_bf16 v[68:71], v[4:7], a[180:183], v[68:71]\n"
    "s_mov_b32 m0, s74\n"
    "s_mov_b32 s100, 12288\n"
    "v_cvt_pk_bf16_f32 v137, v154, v155\n"
    "v_mfma_f32_16x16x32_bf16 v[24:27], v[64:67], a[180:183], v[24:27]\n"
    "ds_read_b128 v[20:23], v205 offset:18432\n"
    "v_cvt_pk_bf16_f32 v138, v156, v157\n"
    "v_mfma_f32_16x16x32_bf16 v[80:83], v[188:191], a[180:183], v[80:83]\n"
    "ds_read_b128 v[76:79], v205 offset:20480\n"
    "v_cvt_pk_bf16_f32 v139, v158, v159\n"
    "v_mfma_f32_16x16x32_bf16 v[228:231], v[96:99], a[204:207], v[228:231]\n"
    "ds_read_b128 v[88:91], v205 offset:22528\n"
    "v_xor_b32 v205, 0x8000, v205\n"
    "v_mfma_f32_16x16x32_bf16 v[172:175], v[4:7], a[204:207], v[172:175]\n"
    "v_permlane16_swap_b32 v252, v204\n"
    "v_mfma_f32_16x16x32_bf16 v[36:39], v[64:67], a[204:207], v[36:39]\n"
    "v_exp_f32 v160, v160\n"
    "v_mfma_f32_16x16x32_bf16 v[168:171], v[188:191], a[204:207], v[168:171]\n"
    "v_exp_f32 v161, v161\n"
    "v_exp_f32 v162, v162\n"
    "v_exp_f32 v163, v163\n"
    "v_exp_f32 v164, v164\n"
    "v_exp_f32 v165, v165\n"
    "v_exp_f32 v166, v166\n"
    "v_exp_f32 v167, v167\n"
    "v_cvt_pk_bf16_f32 v144, v160, v161\n"
    "v_cvt_pk_bf16_f32 v145, v162, v163\n"
    "v_cvt_pk_bf16_f32 v146, v164, v165\n"
    "v_cvt_pk_bf16_f32 v147, v166, v167\n"
    "v_max_f32 v252, v252, v204\n"
    "s_xor_b32 s72, s72, 0x8000\n"
    "v_mul_f32 v252, s71, v252\n"
    "v_mov_b32 v204, v252\n"
    "s_waitcnt vmcnt(6)\n"
    "ds_read_b64_tr_b16 a[160:161], v201 offset:0\n"
    "ds_read_b64_tr_b16 a[162:163], v201 offset:768\n"
    "ds_read_b64_tr_b16 a[184:185], v201 offset:32\n"
    "ds_read_b64_tr_b16 a[186:187], v201 offset:800\n"
    "ds_read_b64_tr_b16 a[208:209], v201 offset:64\n"
    "ds_read_b64_tr_b16 a[210:211], v201 offset:832\n"
    "v_permlane32_swap_b32 v252, v204\n"
    "v_fma_f32 v104, v104, s68, v252\n"
    "v_fma_f32 v105, v105, s68, v252\n"
    "v_fma_f32 v106, v106, s68, v252\n"
    "v_fma_f32 v107, v107, s68, v252\n"
    "v_fma_f32 v120, v120, s68, v252\n"
    "v_fma_f32 v121, v121, s68, v252\n"
    "v_fma_f32 v122, v122, s68, v252\n"
    "v_fma_f32 v123, v123, s68, v252\n"
    "v_exp_f32 v104, v104\n"
    "v_exp_f32 v105, v105\n"
    "v_exp_f32 v106, v106\n"
    "v_exp_f32 v107, v107\n"
    "s_waitcnt lgkmcnt(4)\n"
    "v_mfma_f32_16x16x32_bf16 v[108:111], v[232:235], a[160:163], v[108:111]\n"
    "v_exp_f32 v120, v120\n"
    "v_mfma_f32_16x16x32_bf16 v[132:135], v[20:23], a[160:163], v[132:135]\n"
    "ds_read_b128 v[8:11], v206 offset:16384\n"
    "v_fma_f32 v128, v128, s68, v252\n"
    "v_mfma_f32_16x16x32_bf16 v[236:239], v[76:79], a[160:163], v[236:239]\n"
    "s_waitcnt lgkmcnt(1)\n"
    "buffer_load_dwordx4 v254, s[52:55], s100 offen offset:0 lds\n"
    "v_mfma_f32_16x16x32_bf16 v[100:103], v[88:91], a[160:163], v[100:103]\n"
    "buffer_load_dwordx4 v246, s[52:55], s100 offen offset:1024 lds\n"
    "v_mfma_f32_16x16x32_bf16 v[72:75], v[232:235], a[184:187], v[72:75]\n"
    "buffer_load_dwordx4 v247, s[52:55], s100 offen offset:2048 lds\n"
    "v_mfma_f32_16x16x32_bf16 v[68:71], v[20:23], a[184:187], v[68:71]\n"
    "s_waitcnt vmcnt(6)\n"
    "ds_read_b64_tr_b16 a[164:165], v201 offset:3072\n"
    "s_add_u32 m0, s74, 3072\n"
    "v_mfma_f32_16x16x32_bf16 v[24:27], v[76:79], a[184:187], v[24:27]\n"
    "ds_read_b64_tr_b16 a[166:167], v201 offset:3840\n"
    "s_mov_b32 s100, 24576\n"
    "v_mfma_f32_16x16x32_bf16 v[80:83], v[88:91], a[184:187], v[80:83]\n"
    "ds_read_b64_tr_b16 a[188:189], v201 offset:3104\n"
    "v_fma_f32 v129, v129, s68, v252\n"
    "v_mfma_f32_16x16x32_bf16 v[228:231], v[232:235], a[208:211], v[228:231]\n"
    "ds_read_b64_tr_b16 a[190:191], v201 offset:3872\n"
    "v_fma_f32 v130, v130, s68, v252\n"
    "v_mfma_f32_16x16x32_bf16 v[172:175], v[20:23], a[208:211], v[172:175]\n"
    "ds_read_b64_tr_b16 a[212:213], v201 offset:3136\n"
    "v_fma_f32 v131, v131, s68, v252\n"
    "v_mfma_f32_16x16x32_bf16 v[36:39], v[76:79], a[208:211], v[36:39]\n"
    "ds_read_b64_tr_b16 a[214:215], v201 offset:3904\n"
    "v_fma_f32 v196, v196, s68, v252\n"
    "v_mfma_f32_16x16x32_bf16 v[168:171], v[88:91], a[208:211], v[168:171]\n"
    "ds_read_b128 v[248:251], v206 offset:18432\n"
    "ds_read_b128 v[240:243], v206 offset:20480\n"
    "ds_read_b128 v[0:3], v206 offset:22528\n"
    "v_xor_b32 v206, 0x8000, v206\n"
    "v_exp_f32 v121, v121\n"
    "s_waitcnt lgkmcnt(7)\n"
    "v_mfma_f32_16x16x32_bf16 v[108:111], v[8:11], a[164:167], v[108:111]\n"
    "v_exp_f32 v122, v122\n"
    "v_exp_f32 v123, v123\n"
    "v_fma_f32 v197, v197, s68, v252\n"
    "v_fma_f32 v198, v198, s68, v252\n"
    "v_fma_f32 v199, v199, s68, v252\n"
    "v_cvt_pk_bf16_f32 v76, v104, v105\n"
    "v_cvt_pk_bf16_f32 v77, v106, v107\n"
    "v_cvt_pk_bf16_f32 v78, v120, v121\n"
    "v_cvt_pk_bf16_f32 v88, v108, v109\n"
    "v_cvt_pk_bf16_f32 v89, v110, v111\n"
    "v_cvt_pk_bf16_f32 v79, v122, v123\n"
    "s_waitcnt lgkmcnt(3)\n"
    "buffer_load_dwordx4 v254, s[52:55], s100 offen offset:0 lds\n"
    "s_waitcnt lgkmcnt(2)\n"
    "v_mfma_f32_16x16x32_bf16 v[132:135], v[248:251], a[164:167], v[132:135]\n"
    "buffer_load_dwordx4 v246, s[52:55], s100 offen offset:1024 lds\n"
    "s_waitcnt lgkmcnt(1)\n"
    "v_mfma_f32_16x16x32_bf16 v[236:239], v[240:243], a[164:167], v[236:239]\n"
    "buffer_load_dwordx4 v247, s[52:55], s100 offen offset:2048 lds\n"
    "s_waitcnt lgkmcnt(0)\n"
    "v_mfma_f32_16x16x32_bf16 v[100:103], v[0:3], a[164:167], v[100:103]\n"
    "s_waitcnt vmcnt(6)\n"
    "ds_read_b64_tr_b16 a[216:217], v255 offset:6144\n"
    "s_add_u32 m0, s74, 6144\n"
    "v_mfma_f32_16x16x32_bf16 v[72:75], v[8:11], a[188:191], v[72:75]\n"
    "ds_read_b64_tr_b16 a[218:219], v255 offset:6912\n"
    "s_mov_b32 s100, 36864\n"
    "v_mfma_f32_16x16x32_bf16 v[68:71], v[248:251], a[188:191], v[68:71]\n"
    "ds_read_b64_tr_b16 a[240:241], v255 offset:6152\n"
    "v_cvt_pk_bf16_f32 v90, v132, v133\n"
    "v_mfma_f32_16x16x32_bf16 v[24:27], v[240:243], a[188:191], v[24:27]\n"
    "ds_read_b64_tr_b16 a[242:243], v255 offset:6920\n"
    "v_cvt_pk_bf16_f32 v91, v134, v135\n"
    "v_mfma_f32_16x16x32_bf16 v[80:83], v[0:3], a[188:191], v[80:83]\n"
    "ds_read_b64_tr_b16 v[232:233], v201 offset:6208\n"
    "v_cvt_pk_bf16_f32 v96, v72, v73\n"
    "v_mfma_f32_16x16x32_bf16 v[132:135], v[88:91], v[112:115], 0\n"
    "ds_read_b64_tr_b16 v[234:235], v201 offset:6976\n"
    "v_cvt_pk_bf16_f32 v97, v74, v75\n"
    "v_mfma_f32_16x16x32_bf16 v[228:231], v[8:11], a[212:215], v[228:231]\n"
    "v_cvt_pk_bf16_f32 v98, v68, v69\n"
    "v_cvt_pk_bf16_f32 v99, v70, v71\n"
    "v_mfma_f32_16x16x32_bf16 v[172:175], v[248:251], a[212:215], v[172:175]\n"
    "v_cvt_pk_bf16_f32 v92, v236, v237\n"
    "v_cvt_pk_bf16_f32 v93, v238, v239\n"
    "v_mfma_f32_16x16x32_bf16 v[36:39], v[240:243], a[212:215], v[36:39]\n"
    "v_cvt_pk_bf16_f32 v94, v100, v101\n"
    "v_cvt_pk_bf16_f32 v95, v102, v103\n"
    "v_mfma_f32_16x16x32_bf16 v[168:171], v[0:3], a[212:215], v[168:171]\n"
    "s_barrier\n"
    "v_mfma_f32_16x16x32_bf16 v[132:135], v[92:95], v[140:143], v[132:135]\n"
    "ds_read_b128 v[28:31], v205 offset:0\n"
    "v_cvt_pk_bf16_f32 v104, v228, v229\n"
    "v_mfma_f32_16x16x32_bf16 v[120:123], v[96:99], v[112:115], 0\n"
    "s_waitcnt lgkmcnt(1)\n"
    "buffer_load_dwordx4 v254, s[52:55], s100 offen offset:0 lds\n"
    "buffer_load_dwordx4 v246, s[52:55], s100 offen offset:1024 lds\n"
    "buffer_load_dwordx4 v247, s[52:55], s100 offen offset:2048 lds\n"
    "s_mov_b32 m0, s74\n"
    "s_mov_b32 s100, 49152\n"
    "ds_read_b128 v[148:151], v206 offset:0\n"
    "ds_read_b128 v[176:179], v205 offset:8192\n"
    "ds_read_b128 v[72:75], v206 offset:8192\n"
    "ds_read_b128 v[164:167], v205 offset:16384\n"
    "ds_read_b128 v[152:155], v206 offset:16384\n"
    "v_cvt_pk_bf16_f32 v105, v230, v231\n"
    "s_waitcnt lgkmcnt(5)\n"
    "v_mfma_f32_16x16x32_bf16 v[68:71], a[0:3], v[28:31], 0\n"
    "v_cvt_pk_bf16_f32 v106, v172, v173\n"
    "v_cvt_pk_bf16_f32 v107, v174, v175\n"
    "v_cvt_pk_bf16_f32 v100, v24, v25\n"
    "v_cvt_pk_bf16_f32 v101, v26, v27\n"
    "v_cvt_pk_bf16_f32 v102, v80, v81\n"
    "v_cvt_pk_bf16_f32 v103, v82, v83\n"
    "v_cvt_pk_bf16_f32 v108, v36, v37\n"
    "v_cvt_pk_bf16_f32 v109, v38, v39\n"
    "v_mfma_f32_16x16x32_bf16 v[120:123], v[100:103], v[140:143], v[120:123]\n"
    "v_cvt_pk_bf16_f32 v110, v168, v169\n"
    "v_cvt_pk_bf16_f32 v111, v170, v171\n"
    "v_mfma_f32_16x16x32_bf16 v[156:159], v[104:107], v[112:115], 0\n"
    "v_mul_f32 v132, v132, v253\n"
    "v_mul_f32 v133, v133, v253\n"
    "v_mfma_f32_16x16x32_bf16 v[156:159], v[108:111], v[140:143], v[156:159]\n"
    "v_mul_f32 v134, v134, v253\n"
    "v_mul_f32 v135, v135, v253\n"
    "v_mfma_f32_16x16x32_bf16 v[112:115], v[220:223], v[136:139], 0\n"
    "v_mul_f32 v120, v120, v253\n"
    "v_mul_f32 v121, v121, v253\n"
    "v_mfma_f32_16x16x32_bf16 v[112:115], v[220:223], v[144:147], v[112:115]\n"
    "v_mul_f32 v122, v122, v253\n"
    "v_mul_f32 v123, v123, v253\n"
    "v_mfma_f32_16x16x32_bf16 v[64:67], v[88:91], v[136:139], 0\n"
    "v_mul_f32 v156, v156, v253\n"
    "v_mul_f32 v157, v157, v253\n"
    "v_mfma_f32_16x16x32_bf16 v[64:67], v[92:95], v[144:147], v[64:67]\n"
    "v_mul_f32 v158, v158, v253\n"
    "v_mul_f32 v159, v159, v253\n"
    "v_mfma_f32_16x16x32_bf16 v[224:227], v[96:99], v[136:139], 0\n"
    "v_cvt_pk_bf16_f32 v0, v132, v133\n"
    "v_cvt_pk_bf16_f32 v1, v134, v135\n"
    "v_mfma_f32_16x16x32_bf16 v[224:227], v[100:103], v[144:147], v[224:227]\n"
    "v_cvt_pk_bf16_f32 v2, v120, v121\n"
    "v_cvt_pk_bf16_f32 v3, v122, v123\n"
    "v_mfma_f32_16x16x32_bf16 v[248:251], v[104:107], v[136:139], 0\n"
    "v_cvt_pk_bf16_f32 v4, v156, v157\n"
    "v_cvt_pk_bf16_f32 v5, v158, v159\n"
    "v_mfma_f32_16x16x32_bf16 v[248:251], v[108:111], v[144:147], v[248:251]\n"
    "v_rcp_f32 v253, v112\n"
    "v_mfma_f32_16x16x32_bf16 v[192:195], v[220:223], v[76:79], 0\n"
    "s_waitcnt lgkmcnt(0)\n"
    "v_mul_f32 v64, v64, v253\n"
    "v_mul_f32 v65, v65, v253\n"
    "v_mfma_f32_16x16x32_bf16 v[68:71], a[4:7], v[148:151], v[68:71]\n"
    "s_waitcnt vmcnt(6)\n"
    "ds_read_b64_tr_b16 a[220:221], v255 offset:0\n"
    "v_mul_f32 v66, v66, v253\n"
    "v_mfma_f32_16x16x32_bf16 v[68:71], a[8:11], v[176:179], v[68:71]\n"
    "ds_read_b64_tr_b16 a[222:223], v255 offset:768\n"
    "v_mul_f32 v67, v67, v253\n"
    "v_mfma_f32_16x16x32_bf16 v[68:71], a[12:15], v[72:75], v[68:71]\n"
    "ds_read_b64_tr_b16 a[244:245], v255 offset:8\n"
    "v_mul_f32 v224, v224, v253\n"
    "v_mfma_f32_16x16x32_bf16 v[68:71], a[16:19], v[164:167], v[68:71]\n"
    "ds_read_b64_tr_b16 a[246:247], v255 offset:776\n"
    "v_mul_f32 v225, v225, v253\n"
    "v_mfma_f32_16x16x32_bf16 v[68:71], a[20:23], v[152:155], v[68:71]\n"
    "ds_read_b64_tr_b16 v[236:237], v201 offset:64\n"
    "v_mul_f32 v226, v226, v253\n"
    "v_mfma_f32_16x16x32_bf16 v[112:115], a[24:27], v[28:31], 0\n"
    "ds_read_b64_tr_b16 v[238:239], v201 offset:832\n"
    "v_mul_f32 v227, v227, v253\n"
    "v_mfma_f32_16x16x32_bf16 v[112:115], a[28:31], v[148:151], v[112:115]\n"
    "v_mul_f32 v248, v248, v253\n"
    "v_mul_f32 v249, v249, v253\n"
    "v_mfma_f32_16x16x32_bf16 v[112:115], a[32:35], v[176:179], v[112:115]\n"
    "v_cvt_pk_bf16_f32 v36, v68, v69\n"
    "v_cvt_pk_bf16_f32 v37, v70, v71\n"
    "v_mfma_f32_16x16x32_bf16 v[112:115], a[36:39], v[72:75], v[112:115]\n"
    "v_mul_f32 v250, v250, v253\n"
    "v_mul_f32 v251, v251, v253\n"
    "v_mfma_f32_16x16x32_bf16 v[112:115], a[40:43], v[164:167], v[112:115]\n"
    "v_cvt_pk_bf16_f32 v6, v64, v65\n"
    "v_cvt_pk_bf16_f32 v7, v66, v67\n"
    "v_mfma_f32_16x16x32_bf16 v[112:115], a[44:47], v[152:155], v[112:115]\n"
    "v_cvt_pk_bf16_f32 v8, v224, v225\n"
    "v_cvt_pk_bf16_f32 v9, v226, v227\n"
    "v_mfma_f32_16x16x32_bf16 v[144:147], a[48:51], v[28:31], 0\n"
    "v_cvt_pk_bf16_f32 v10, v248, v249\n"
    "v_cvt_pk_bf16_f32 v11, v250, v251\n"
    "v_mfma_f32_16x16x32_bf16 v[144:147], a[52:55], v[148:151], v[144:147]\n"
    "s_waitcnt vmcnt(3)\n"
    "ds_read_b64_tr_b16 a[224:225], v255 offset:3072\n"
    "v_fma_f32 v16, v16, s68, v204\n"
    "v_mfma_f32_16x16x32_bf16 v[144:147], a[56:59], v[176:179], v[144:147]\n"
    "s_waitcnt lgkmcnt(1)\n"
    "buffer_load_dwordx4 v254, s[52:55], s100 offen offset:0 lds\n"
    "v_mfma_f32_16x16x32_bf16 v[144:147], a[60:63], v[72:75], v[144:147]\n"
    "buffer_load_dwordx4 v246, s[52:55], s100 offen offset:1024 lds\n"
    "v_mfma_f32_16x16x32_bf16 v[144:147], a[64:67], v[164:167], v[144:147]\n"
    "buffer_load_dwordx4 v247, s[52:55], s100 offen offset:2048 lds\n"
    "v_mfma_f32_16x16x32_bf16 v[144:147], a[68:71], v[152:155], v[144:147]\n"
    "ds_read_b64_tr_b16 a[226:227], v255 offset:3840\n"
    "s_add_u32 m0, s74, 3072\n"
    "v_mfma_f32_16x16x32_bf16 v[120:123], a[72:75], v[28:31], 0\n"
    "ds_read_b64_tr_b16 a[248:249], v255 offset:3080\n"
    "s_mov_b32 s100, 61440\n"
    "v_mfma_f32_16x16x32_bf16 v[120:123], a[76:79], v[148:151], v[120:123]\n"
    "ds_read_b64_tr_b16 a[250:251], v255 offset:3848\n"
    "v_cvt_pk_bf16_f32 v38, v112, v113\n"
    "v_mfma_f32_16x16x32_bf16 v[120:123], a[80:83], v[176:179], v[120:123]\n"
    "ds_read_b64_tr_b16 v[240:241], v201 offset:3136\n"
    "v_cvt_pk_bf16_f32 v39, v114, v115\n"
    "v_mfma_f32_16x16x32_bf16 v[120:123], a[84:87], v[72:75], v[120:123]\n"
    "ds_read_b64_tr_b16 v[242:243], v201 offset:3904\n"
    "v_cvt_pk_bf16_f32 v52, v144, v145\n"
    "v_mfma_f32_16x16x32_bf16 v[120:123], a[88:91], v[164:167], v[120:123]\n"
    "v_cvt_pk_bf16_f32 v53, v146, v147\n"
    "v_fma_f32 v17, v17, s68, v204\n"
    "v_mfma_f32_16x16x32_bf16 v[120:123], a[92:95], v[152:155], v[120:123]\n"
    "v_exp_f32 v128, v128\n"
    "v_mfma_f32_16x16x32_bf16 v[136:139], a[96:99], v[28:31], 0\n"
    "v_exp_f32 v129, v129\n"
    "v_mfma_f32_16x16x32_bf16 v[136:139], a[100:103], v[148:151], v[136:139]\n"
    "v_exp_f32 v130, v130\n"
    "v_mfma_f32_16x16x32_bf16 v[136:139], a[104:107], v[176:179], v[136:139]\n"
    "s_waitcnt vmcnt(3)\n"
    "ds_read_b64_tr_b16 a[228:229], v255 offset:6144\n"
    "v_cvt_pk_bf16_f32 v128, v128, v129\n"
    "v_mfma_f32_16x16x32_bf16 v[136:139], a[108:111], v[72:75], v[136:139]\n"
    "ds_read_b64_tr_b16 a[230:231], v255 offset:6912\n"
    "v_cvt_pk_bf16_f32 v64, v120, v121\n"
    "v_mfma_f32_16x16x32_bf16 v[136:139], a[112:115], v[164:167], v[136:139]\n"
    "ds_read_b64_tr_b16 a[252:253], v255 offset:6152\n"
    "v_cvt_pk_bf16_f32 v65, v122, v123\n"
    "v_mfma_f32_16x16x32_bf16 v[136:139], a[116:119], v[152:155], v[136:139]\n"
    "s_waitcnt lgkmcnt(3)\n"
    "buffer_load_dwordx4 v254, s[52:55], s100 offen offset:0 lds\n"
    "v_mfma_f32_16x16x32_bf16 v[168:171], a[120:123], v[28:31], 0\n"
    "buffer_load_dwordx4 v246, s[52:55], s100 offen offset:1024 lds\n"
    "v_mfma_f32_16x16x32_bf16 v[168:171], a[124:127], v[148:151], v[168:171]\n"
    "buffer_load_dwordx4 v247, s[52:55], s100 offen offset:2048 lds\n"
    "v_mfma_f32_16x16x32_bf16 v[168:171], a[128:131], v[176:179], v[168:171]\n"
    "ds_read_b64_tr_b16 a[254:255], v255 offset:6920\n"
    "v_fma_f32 v18, v18, s68, v204\n"
    "v_mfma_f32_16x16x32_bf16 v[168:171], a[132:135], v[72:75], v[168:171]\n"
    "ds_read_b64_tr_b16 v[244:245], v201 offset:6208\n"
    "v_cvt_pk_bf16_f32 v66, v136, v137\n"
    "v_mfma_f32_16x16x32_bf16 v[168:171], a[136:139], v[164:167], v[168:171]\n"
    "ds_read_b64_tr_b16 v[246:247], v201 offset:6976\n"
    "v_cvt_pk_bf16_f32 v67, v138, v139\n"
    "v_mfma_f32_16x16x32_bf16 v[168:171], a[140:143], v[152:155], v[168:171]\n"
    "ds_read_b128 v[44:47], v205 offset:2048\n"
    "v_fma_f32 v19, v19, s68, v204\n"
    "v_mfma_f32_16x16x32_bf16 v[224:227], v[28:31], a[144:147], 0\n"
    "ds_read_b128 v[12:15], v206 offset:2048\n"
    "v_fma_f32 v116, v116, s68, v204\n"
    "v_mfma_f32_16x16x32_bf16 v[224:227], v[148:151], a[148:151], v[224:227]\n"
    "ds_read_b128 v[132:135], v205 offset:10240\n"
    "v_fma_f32 v117, v117, s68, v204\n"
    "v_mfma_f32_16x16x32_bf16 v[224:227], v[176:179], a[152:155], v[224:227]\n"
    "ds_read_b128 v[136:139], v206 offset:10240\n"
    "v_cvt_pk_bf16_f32 v80, v168, v169\n"
    "v_mfma_f32_16x16x32_bf16 v[224:227], v[72:75], a[156:159], v[224:227]\n"
    "ds_read_b128 v[144:147], v205 offset:18432\n"
    "v_cvt_pk_bf16_f32 v81, v170, v171\n"
    "v_mfma_f32_16x16x32_bf16 v[224:227], v[164:167], a[160:163], v[224:227]\n"
    "ds_read_b128 v[32:35], v206 offset:18432\n"
    "v_fma_f32 v118, v118, s68, v204\n"
    "v_mfma_f32_16x16x32_bf16 v[224:227], v[152:155], a[164:167], v[224:227]\n"
    "v_exp_f32 v131, v131\n"
    "v_mfma_f32_16x16x32_bf16 v[172:175], v[28:31], a[168:171], 0\n"
    "v_exp_f32 v196, v196\n"
    "v_mfma_f32_16x16x32_bf16 v[172:175], v[148:151], a[172:175], v[172:175]\n"
    "v_exp_f32 v197, v197\n"
    "v_mfma_f32_16x16x32_bf16 v[172:175], v[176:179], a[176:179], v[172:175]\n"
    "v_exp_f32 v198, v198\n"
    "v_mfma_f32_16x16x32_bf16 v[172:175], v[72:75], a[180:183], v[172:175]\n"
    "v_cvt_pk_bf16_f32 v112, v224, v225\n"
    "v_cvt_pk_bf16_f32 v113, v226, v227\n"
    "v_mfma_f32_16x16x32_bf16 v[172:175], v[164:167], a[184:187], v[172:175]\n"
    "v_exp_f32 v199, v199\n"
    "v_mfma_f32_16x16x32_bf16 v[172:175], v[152:155], a[188:191], v[172:175]\n"
    "v_cvt_pk_bf16_f32 v129, v130, v131\n"
    "v_cvt_pk_bf16_f32 v130, v196, v197\n"
    "v_mfma_f32_16x16x32_bf16 v[228:231], v[28:31], a[192:195], 0\n"
    "v_cvt_pk_bf16_f32 v131, v198, v199\n"
    "v_fma_f32 v119, v119, s68, v204\n"
    "v_mfma_f32_16x16x32_bf16 v[228:231], v[148:151], a[196:199], v[228:231]\n"
    "v_exp_f32 v16, v16\n"
    "v_mfma_f32_16x16x32_bf16 v[192:195], v[220:223], v[128:131], v[192:195]\n"
    "v_cvt_pk_bf16_f32 v120, v172, v173\n"
    "v_cvt_pk_bf16_f32 v121, v174, v175\n"
    "v_mfma_f32_16x16x32_bf16 v[140:143], v[88:91], v[76:79], 0\n"
    "v_exp_f32 v17, v17\n"
    "v_mfma_f32_16x16x32_bf16 v[140:143], v[92:95], v[128:131], v[140:143]\n"
    "v_exp_f32 v18, v18\n"
    "v_mfma_f32_16x16x32_bf16 v[148:151], v[96:99], v[76:79], 0\n"
    "v_exp_f32 v19, v19\n"
    "v_mfma_f32_16x16x32_bf16 v[148:151], v[100:103], v[128:131], v[148:151]\n"
    "v_rcp_f32 v254, v192\n"
    "v_mfma_f32_16x16x32_bf16 v[28:31], v[104:107], v[76:79], 0\n"
    "v_exp_f32 v116, v116\n"
    "v_mfma_f32_16x16x32_bf16 v[28:31], v[108:111], v[128:131], v[28:31]\n"
    "v_mul_f32 v140, v140, v254\n"
    "v_mul_f32 v141, v141, v254\n"
    "v_mfma_f32_16x16x32_bf16 v[228:231], v[176:179], a[200:203], v[228:231]\n"
    "v_mul_f32 v142, v142, v254\n"
    "v_mul_f32 v143, v143, v254\n"
    "v_mfma_f32_16x16x32_bf16 v[228:231], v[72:75], a[204:207], v[228:231]\n"
    "v_mul_f32 v148, v148, v254\n"
    "v_mul_f32 v149, v149, v254\n"
    "v_mfma_f32_16x16x32_bf16 v[228:231], v[164:167], a[208:211], v[228:231]\n"
    "s_waitcnt vmcnt(3)\n"
    "ds_read_b64_tr_b16 a[232:233], v255 offset:0\n"
    "v_mul_f32 v150, v150, v254\n"
    "v_mfma_f32_16x16x32_bf16 v[228:231], v[152:155], a[212:215], v[228:231]\n"
    "ds_read_b64_tr_b16 a[234:235], v255 offset:768\n"
    "v_mul_f32 v151, v151, v254\n"
    "s_waitcnt lgkmcnt(2)\n"
    "v_mfma_f32_16x16x32_bf16 v[152:155], a[0:3], v[44:47], 0\n"
    "ds_read_b64_tr_b16 v[224:225], v255 offset:8\n"
    "v_mul_f32 v28, v28, v254\n"
    "v_mfma_f32_16x16x32_bf16 v[152:155], a[4:7], v[12:15], v[152:155]\n"
    "ds_read_b64_tr_b16 v[226:227], v255 offset:776\n"
    "v_mul_f32 v29, v29, v254\n"
    "v_mfma_f32_16x16x32_bf16 v[152:155], a[8:11], v[132:135], v[152:155]\n"
    "ds_read_b64_tr_b16 v[248:249], v201 offset:64\n"
    "v_cvt_pk_bf16_f32 v128, v228, v229\n"
    "v_mfma_f32_16x16x32_bf16 v[152:155], a[12:15], v[136:139], v[152:155]\n"
    "ds_read_b64_tr_b16 v[250:251], v201 offset:832\n"
    "v_cvt_pk_bf16_f32 v129, v230, v231\n"
    "v_mfma_f32_16x16x32_bf16 v[152:155], a[16:19], v[144:147], v[152:155]\n"
    "v_mul_f32 v30, v30, v254\n"
    "v_mul_f32 v31, v31, v254\n"
    "v_mfma_f32_16x16x32_bf16 v[152:155], a[20:23], v[32:35], v[152:155]\n"
    "v_cvt_pk_bf16_f32 v20, v140, v141\n"
    "v_cvt_pk_bf16_f32 v21, v142, v143\n"
    "v_mfma_f32_16x16x32_bf16 v[156:159], a[24:27], v[44:47], 0\n"
    "v_cvt_pk_bf16_f32 v22, v148, v149\n"
    "v_cvt_pk_bf16_f32 v23, v150, v151\n"
    "v_mfma_f32_16x16x32_bf16 v[156:159], a[28:31], v[12:15], v[156:159]\n"
    "v_cvt_pk_bf16_f32 v24, v28, v29\n"
    "v_cvt_pk_bf16_f32 v25, v30, v31\n"
    "v_mfma_f32_16x16x32_bf16 v[156:159], a[32:35], v[132:135], v[156:159]\n"
    "v_cvt_pk_bf16_f32 v40, v152, v153\n"
    "v_cvt_pk_bf16_f32 v41, v154, v155\n"
    "v_mfma_f32_16x16x32_bf16 v[156:159], a[36:39], v[136:139], v[156:159]\n"
    "v_exp_f32 v117, v117\n"
    "v_mfma_f32_16x16x32_bf16 v[156:159], a[40:43], v[144:147], v[156:159]\n"
    "v_exp_f32 v118, v118\n"
    "v_mfma_f32_16x16x32_bf16 v[156:159], a[44:47], v[32:35], v[156:159]\n"
    "v_exp_f32 v119, v119\n"
    "v_mfma_f32_16x16x32_bf16 v[196:199], a[48:51], v[44:47], 0\n"
    "v_fma_f32 v124, v124, s68, v204\n"
    "v_fma_f32 v125, v125, s68, v204\n"
    "v_mfma_f32_16x16x32_bf16 v[196:199], a[52:55], v[12:15], v[196:199]\n"
    "v_fma_f32 v126, v126, s68, v204\n"
    "v_fma_f32 v127, v127, s68, v204\n"
    "v_mfma_f32_16x16x32_bf16 v[196:199], a[56:59], v[132:135], v[196:199]\n"
    "s_waitcnt vmcnt(0)\n"
    "ds_read_b64_tr_b16 a[236:237], v255 offset:3072\n"
    "v_cvt_pk_bf16_f32 v42, v156, v157\n"
    "v_mfma_f32_16x16x32_bf16 v[196:199], a[60:63], v[136:139], v[196:199]\n"
    "ds_read_b64_tr_b16 a[238:239], v255 offset:3840\n"
    "v_cvt_pk_bf16_f32 v43, v158, v159\n"
    "v_mfma_f32_16x16x32_bf16 v[196:199], a[64:67], v[144:147], v[196:199]\n"
    "ds_read_b64_tr_b16 v[228:229], v255 offset:3080\n"
    "v_fma_f32 v84, v84, s68, v204\n"
    "v_mfma_f32_16x16x32_bf16 v[196:199], a[68:71], v[32:35], v[196:199]\n"
    "ds_read_b64_tr_b16 v[230:231], v255 offset:3848\n"
    "v_fma_f32 v85, v85, s68, v204\n"
    "v_mfma_f32_16x16x32_bf16 v[28:31], a[72:75], v[44:47], 0\n"
    "ds_read_b64_tr_b16 v[252:253], v201 offset:3136\n"
    "v_fma_f32 v86, v86, s68, v204\n"
    "v_mfma_f32_16x16x32_bf16 v[28:31], a[76:79], v[12:15], v[28:31]\n"
    "ds_read_b64_tr_b16 v[254:255], v201 offset:3904\n"
    "v_fma_f32 v87, v87, s68, v204\n"
    "v_mfma_f32_16x16x32_bf16 v[28:31], a[80:83], v[132:135], v[28:31]\n"
    "v_cvt_pk_bf16_f32 v56, v196, v197\n"
    "v_cvt_pk_bf16_f32 v57, v198, v199\n"
    "v_mfma_f32_16x16x32_bf16 v[28:31], a[84:87], v[136:139], v[28:31]\n"
    "v_cvt_pk_bf16_f32 v160, v16, v17\n"
    "v_cvt_pk_bf16_f32 v161, v18, v19\n"
    "v_mfma_f32_16x16x32_bf16 v[28:31], a[88:91], v[144:147], v[28:31]\n"
    "v_cvt_pk_bf16_f32 v162, v116, v117\n"
    "v_cvt_pk_bf16_f32 v163, v118, v119\n"
    "v_mfma_f32_16x16x32_bf16 v[28:31], a[92:95], v[32:35], v[28:31]\n"
    "v_exp_f32 v124, v124\n"
    "v_mfma_f32_16x16x32_bf16 v[116:119], v[220:223], v[160:163], 0\n"
    "v_exp_f32 v125, v125\n"
    "v_mfma_f32_16x16x32_bf16 v[164:167], a[96:99], v[44:47], 0\n"
    "v_exp_f32 v126, v126\n"
    "v_mfma_f32_16x16x32_bf16 v[164:167], a[100:103], v[12:15], v[164:167]\n"
    "v_exp_f32 v127, v127\n"
    "v_mfma_f32_16x16x32_bf16 v[164:167], a[104:107], v[132:135], v[164:167]\n"
    "v_cvt_pk_bf16_f32 v68, v28, v29\n"
    "v_cvt_pk_bf16_f32 v69, v30, v31\n"
    "v_mfma_f32_16x16x32_bf16 v[164:167], a[108:111], v[136:139], v[164:167]\n"
    "v_exp_f32 v84, v84\n"
    "v_mfma_f32_16x16x32_bf16 v[164:167], a[112:115], v[144:147], v[164:167]\n"
    "v_exp_f32 v85, v85\n"
    "v_mfma_f32_16x16x32_bf16 v[164:167], a[116:119], v[32:35], v[164:167]\n"
    "v_exp_f32 v86, v86\n"
    "v_mfma_f32_16x16x32_bf16 v[140:143], a[120:123], v[44:47], 0\n"
    "v_exp_f32 v87, v87\n"
    "v_mfma_f32_16x16x32_bf16 v[140:143], a[124:127], v[12:15], v[140:143]\n"
    "v_cvt_pk_bf16_f32 v124, v124, v125\n"
    "v_cvt_pk_bf16_f32 v125, v126, v127\n"
    "v_mfma_f32_16x16x32_bf16 v[140:143], a[128:131], v[132:135], v[140:143]\n"
    "v_cvt_pk_bf16_f32 v126, v84, v85\n"
    "v_cvt_pk_bf16_f32 v70, v164, v165\n"
    "v_mfma_f32_16x16x32_bf16 v[140:143], a[132:135], v[136:139], v[140:143]\n"
    "v_cvt_pk_bf16_f32 v71, v166, v167\n"
    "v_cvt_pk_bf16_f32 v127, v86, v87\n"
    "v_mfma_f32_16x16x32_bf16 v[140:143], a[136:139], v[144:147], v[140:143]\n"
    "ds_read_b128 v[16:19], v205 offset:4096\n"
    "v_mov_b64 v[168:169], v[80:81]\n"
    "v_mfma_f32_16x16x32_bf16 v[116:119], v[220:223], v[124:127], v[116:119]\n"
    "v_mov_b64 v[174:175], v[80:81]\n"
    "v_mov_b64 v[180:181], v[80:81]\n"
    "v_mfma_f32_16x16x32_bf16 v[84:87], v[88:91], v[160:163], 0\n"
    "v_mfma_f32_16x16x32_bf16 v[84:87], v[92:95], v[124:127], v[84:87]\n"
    "v_mfma_f32_16x16x32_bf16 v[192:195], v[96:99], v[160:163], 0\n"
    "v_mfma_f32_16x16x32_bf16 v[192:195], v[100:103], v[124:127], v[192:195]\n"
    "v_mfma_f32_16x16x32_bf16 v[48:51], v[104:107], v[160:163], 0\n"
    "v_mfma_f32_16x16x32_bf16 v[48:51], v[108:111], v[124:127], v[48:51]\n"
    "v_rcp_f32 v204, v116\n"
    "v_mfma_f32_16x16x32_bf16 v[140:143], a[140:143], v[32:35], v[140:143]\n"
    "ds_read_b128 v[108:111], v206 offset:4096\n"
    "v_mfma_f32_16x16x32_bf16 v[124:127], v[44:47], a[144:147], 0\n"
    "ds_read_b128 v[28:31], v205 offset:12288\n"
    "v_mul_f32 v84, v84, v204\n"
    "v_mfma_f32_16x16x32_bf16 v[124:127], v[12:15], a[148:151], v[124:127]\n"
    "ds_read_b128 v[88:91], v206 offset:12288\n"
    "v_mul_f32 v85, v85, v204\n"
    "v_mfma_f32_16x16x32_bf16 v[124:127], v[132:135], a[152:155], v[124:127]\n"
    "ds_read_b128 v[76:79], v205 offset:20480\n"
    "v_cvt_pk_bf16_f32 v82, v140, v141\n"
    "v_mfma_f32_16x16x32_bf16 v[124:127], v[136:139], a[156:159], v[124:127]\n"
    "ds_read_b128 v[148:151], v206 offset:20480\n"
    "v_cvt_pk_bf16_f32 v83, v142, v143\n"
    "v_mfma_f32_16x16x32_bf16 v[124:127], v[144:147], a[160:163], v[124:127]\n"
    "v_mul_f32 v86, v86, v204\n"
    "v_mul_f32 v87, v87, v204\n"
    "v_mfma_f32_16x16x32_bf16 v[124:127], v[32:35], a[164:167], v[124:127]\n"
    "v_mul_f32 v192, v192, v204\n"
    "v_mul_f32 v193, v193, v204\n"
    "v_mfma_f32_16x16x32_bf16 v[72:75], v[44:47], a[168:171], 0\n"
    "v_mul_f32 v194, v194, v204\n"
    "v_mul_f32 v195, v195, v204\n"
    "v_mfma_f32_16x16x32_bf16 v[72:75], v[12:15], a[172:175], v[72:75]\n"
    "v_mul_f32 v48, v48, v204\n"
    "v_mul_f32 v49, v49, v204\n"
    "v_mfma_f32_16x16x32_bf16 v[72:75], v[132:135], a[176:179], v[72:75]\n"
    "v_cvt_pk_bf16_f32 v114, v124, v125\n"
    "v_cvt_pk_bf16_f32 v115, v126, v127\n"
    "v_mfma_f32_16x16x32_bf16 v[72:75], v[136:139], a[180:183], v[72:75]\n"
    "v_mul_f32 v50, v50, v204\n"
    "v_mul_f32 v51, v51, v204\n"
    "v_mfma_f32_16x16x32_bf16 v[72:75], v[144:147], a[184:187], v[72:75]\n"
    "v_cvt_pk_bf16_f32 v26, v84, v85\n"
    "v_cvt_pk_bf16_f32 v27, v86, v87\n"
    "v_mfma_f32_16x16x32_bf16 v[72:75], v[32:35], a[188:191], v[72:75]\n"
    "v_cvt_pk_bf16_f32 v62, v192, v193\n"
    "v_cvt_pk_bf16_f32 v63, v194, v195\n"
    "v_mfma_f32_16x16x32_bf16 v[84:87], v[44:47], a[192:195], 0\n"
    "v_cvt_pk_bf16_f32 v48, v48, v49\n"
    "v_cvt_pk_bf16_f32 v49, v50, v51\n"
    "v_mfma_f32_16x16x32_bf16 v[84:87], v[12:15], a[196:199], v[84:87]\n"
    "v_mov_b64 v[182:183], v[82:83]\n"
    "v_mov_b64 v[188:189], v[82:83]\n"
    "v_mfma_f32_16x16x32_bf16 v[84:87], v[132:135], a[200:203], v[84:87]\n"
    "v_cvt_pk_bf16_f32 v122, v72, v73\n"
    "v_cvt_pk_bf16_f32 v123, v74, v75\n"
    "v_mfma_f32_16x16x32_bf16 v[84:87], v[136:139], a[204:207], v[84:87]\n"
    "v_mov_b64 v[194:195], v[82:83]\n"
    "v_mfma_f32_16x16x32_bf16 v[84:87], v[144:147], a[208:211], v[84:87]\n"
    "v_mfma_f32_16x16x32_bf16 v[84:87], v[32:35], a[212:215], v[84:87]\n"
    "s_waitcnt lgkmcnt(0)\n"
    "s_barrier\n"
    "v_mfma_f32_16x16x32_bf16 v[12:15], a[0:3], v[16:19], 0\n"
    "ds_write_b64 v211, v[0:1] offset:0\n"
    "v_mfma_f32_16x16x32_bf16 v[12:15], a[4:7], v[108:111], v[12:15]\n"
    "s_mov_b32 m0, s73\n"
    "s_xor_b32 s73, s73, 0x8000\n"
    "v_mfma_f32_16x16x32_bf16 v[12:15], a[8:11], v[28:31], v[12:15]\n"
    "buffer_load_dwordx4 v218, s[40:43], s97 offen offset:0 nt lds\n"
    "v_mfma_f32_16x16x32_bf16 v[12:15], a[12:15], v[88:91], v[12:15]\n"
    "v_cvt_pk_bf16_f32 v130, v84, v85\n"
    "v_cvt_pk_bf16_f32 v131, v86, v87\n"
    "v_mfma_f32_16x16x32_bf16 v[12:15], a[16:19], v[76:79], v[12:15]\n"
    "v_mfma_f32_16x16x32_bf16 v[12:15], a[20:23], v[148:151], v[12:15]\n"
    "ds_write_b64 v212, v[2:3] offset:0\n"
    "v_mfma_f32_16x16x32_bf16 v[72:75], a[24:27], v[16:19], 0\n"
    "v_mfma_f32_16x16x32_bf16 v[72:75], a[28:31], v[108:111], v[72:75]\n"
    "buffer_load_dwordx4 v219, s[40:43], s97 offen offset:1024 nt lds\n"
    "v_mfma_f32_16x16x32_bf16 v[72:75], a[32:35], v[28:31], v[72:75]\n"
    "s_mov_b32 m0, s72\n"
    "v_mfma_f32_16x16x32_bf16 v[72:75], a[36:39], v[88:91], v[72:75]\n"
    "buffer_load_dwordx4 v216, s[36:39], s94 offen offset:0 nt lds\n"
    "v_mfma_f32_16x16x32_bf16 v[72:75], a[40:43], v[76:79], v[72:75]\n"
    "v_cvt_pk_bf16_f32 v44, v12, v13\n"
    "v_cvt_pk_bf16_f32 v45, v14, v15\n"
    "v_mfma_f32_16x16x32_bf16 v[72:75], a[44:47], v[148:151], v[72:75]\n"
    "v_mfma_f32_16x16x32_bf16 v[184:187], a[48:51], v[16:19], 0\n"
    "ds_write_b64 v213, v[4:5] offset:0\n"
    "v_mfma_f32_16x16x32_bf16 v[184:187], a[52:55], v[108:111], v[184:187]\n"
    "buffer_load_dwordx4 v217, s[36:39], s94 offen offset:1024 nt lds\n"
    "v_mfma_f32_16x16x32_bf16 v[184:187], a[56:59], v[28:31], v[184:187]\n"
    "v_mfma_f32_16x16x32_bf16 v[184:187], a[60:63], v[88:91], v[184:187]\n"
    "v_mfma_f32_16x16x32_bf16 v[184:187], a[64:67], v[76:79], v[184:187]\n"
    "v_mfma_f32_16x16x32_bf16 v[184:187], a[68:71], v[148:151], v[184:187]\n"
    "v_cvt_pk_bf16_f32 v46, v72, v73\n"
    "v_cvt_pk_bf16_f32 v47, v74, v75\n"
    "v_mfma_f32_16x16x32_bf16 v[156:159], a[72:75], v[16:19], 0\n"
    "ds_write_b64 v211, v[6:7] offset:2048\n"
    "v_mfma_f32_16x16x32_bf16 v[156:159], a[76:79], v[108:111], v[156:159]\n"
    "s_add_u32 m0, s72, 8192\n"
    "LPG0%=:\n"
    "s_cbranch_execz LWB%=\n"
    "s_add_u32 s101, s94, 128\n"
    "v_mfma_f32_16x16x32_bf16 v[156:159], a[80:83], v[28:31], v[156:159]\n"
    "buffer_load_dwordx4 v216, s[36:39], s101 offen offset:0 nt lds\n"
    "v_mfma_f32_16x16x32_bf16 v[156:159], a[84:87], v[88:91], v[156:159]\n"
    "v_cvt_pk_bf16_f32 v58, v184, v185\n"
    "v_cvt_pk_bf16_f32 v59, v186, v187\n"
    "v_mfma_f32_16x16x32_bf16 v[156:159], a[88:91], v[76:79], v[156:159]\n"
    "buffer_load_dwordx4 v217, s[36:39], s101 offen offset:1024 nt lds\n"
    "v_mfma_f32_16x16x32_bf16 v[156:159], a[92:95], v[148:151], v[156:159]\n"
    "v_mfma_f32_16x16x32_bf16 v[0:3], a[96:99], v[16:19], 0\n"
    "v_mfma_f32_16x16x32_bf16 v[0:3], a[100:103], v[108:111], v[0:3]\n"
    "v_mfma_f32_16x16x32_bf16 v[0:3], a[104:107], v[28:31], v[0:3]\n"
    "v_mfma_f32_16x16x32_bf16 v[0:3], a[108:111], v[88:91], v[0:3]\n"
    "ds_write_b64 v212, v[8:9] offset:2048\n"
    "v_mfma_f32_16x16x32_bf16 v[0:3], a[112:115], v[76:79], v[0:3]\n"
    "s_add_u32 m0, s72, 16384\n"
    "s_add_u32 s101, s94, 256\n"
    "v_cvt_pk_bf16_f32 v72, v156, v157\n"
    "v_mfma_f32_16x16x32_bf16 v[0:3], a[116:119], v[148:151], v[0:3]\n"
    "ds_write_b64 v213, v[10:11] offset:2048\n"
    "v_mfma_f32_16x16x32_bf16 v[12:15], a[120:123], v[16:19], 0\n"
    "buffer_load_dwordx4 v216, s[36:39], s101 offen offset:0 nt lds\n"
    "v_mfma_f32_16x16x32_bf16 v[12:15], a[124:127], v[108:111], v[12:15]\n"
    "v_cvt_pk_bf16_f32 v73, v158, v159\n"
    "s_xor_b32 s72, s72, 0x8000\n"
    "v_mfma_f32_16x16x32_bf16 v[12:15], a[128:131], v[28:31], v[12:15]\n"
    "buffer_load_dwordx4 v217, s[36:39], s101 offen offset:1024 nt lds\n"
    "v_mfma_f32_16x16x32_bf16 v[12:15], a[132:135], v[88:91], v[12:15]\n"
    "ds_read_b128 v[92:95], v205 offset:6144\n"
    "v_cvt_pk_bf16_f32 v74, v0, v1\n"
    "v_mfma_f32_16x16x32_bf16 v[12:15], a[136:139], v[76:79], v[12:15]\n"
    "ds_read_b128 v[200:203], v206 offset:6144\n"
    "v_cvt_pk_bf16_f32 v75, v2, v3\n"
    "v_mfma_f32_16x16x32_bf16 v[12:15], a[140:143], v[148:151], v[12:15]\n"
    "ds_write_b64 v211, v[20:21] offset:4096\n"
    "v_mfma_f32_16x16x32_bf16 v[0:3], v[16:19], a[144:147], 0\n"
    "ds_read_b128 v[32:35], v205 offset:14336\n"
    "v_mfma_f32_16x16x32_bf16 v[0:3], v[108:111], a[148:151], v[0:3]\n"
    "ds_read_b128 v[4:7], v206 offset:14336\n"
    "v_mfma_f32_16x16x32_bf16 v[0:3], v[28:31], a[152:155], v[0:3]\n"
    "ds_write_b64 v212, v[22:23] offset:4096\n"
    "v_mfma_f32_16x16x32_bf16 v[0:3], v[88:91], a[156:159], v[0:3]\n"
    "ds_read_b128 v[96:99], v205 offset:22528\n"
    "v_cvt_pk_bf16_f32 v84, v12, v13\n"
    "v_mfma_f32_16x16x32_bf16 v[0:3], v[76:79], a[160:163], v[0:3]\n"
    "ds_read_b128 v[8:11], v206 offset:22528\n"
    "v_cvt_pk_bf16_f32 v85, v14, v15\n"
    "v_mfma_f32_16x16x32_bf16 v[0:3], v[148:151], a[164:167], v[0:3]\n"
    "ds_write_b64 v213, v[24:25] offset:4096\n"
    "v_mfma_f32_16x16x32_bf16 v[12:15], v[16:19], a[168:171], 0\n"
    "v_mfma_f32_16x16x32_bf16 v[12:15], v[108:111], a[172:175], v[12:15]\n"
    "v_mfma_f32_16x16x32_bf16 v[12:15], v[28:31], a[176:179], v[12:15]\n"
    "v_mfma_f32_16x16x32_bf16 v[12:15], v[88:91], a[180:183], v[12:15]\n"
    "v_mfma_f32_16x16x32_bf16 v[12:15], v[76:79], a[184:187], v[12:15]\n"
    "ds_write_b64 v211, v[26:27] offset:6144\n"
    "v_mfma_f32_16x16x32_bf16 v[12:15], v[148:151], a[188:191], v[12:15]\n"
    "v_cvt_pk_bf16_f32 v116, v0, v1\n"
    "v_cvt_pk_bf16_f32 v117, v2, v3\n"
    "v_mfma_f32_16x16x32_bf16 v[132:135], v[16:19], a[192:195], 0\n"
    "v_mfma_f32_16x16x32_bf16 v[132:135], v[108:111], a[196:199], v[132:135]\n"
    "ds_write_b64 v212, v[62:63] offset:6144\n"
    "v_mfma_f32_16x16x32_bf16 v[132:135], v[28:31], a[200:203], v[132:135]\n"
    "v_mfma_f32_16x16x32_bf16 v[132:135], v[88:91], a[204:207], v[132:135]\n"
    "v_mfma_f32_16x16x32_bf16 v[132:135], v[76:79], a[208:211], v[132:135]\n"
    "v_cvt_pk_bf16_f32 v124, v12, v13\n"
    "v_cvt_pk_bf16_f32 v125, v14, v15\n"
    "v_mfma_f32_16x16x32_bf16 v[132:135], v[148:151], a[212:215], v[132:135]\n"
    "ds_write_b64 v213, v[48:49] offset:6144\n"
    "s_waitcnt lgkmcnt(3)\n"
    "v_mfma_f32_16x16x32_bf16 v[196:199], a[0:3], v[92:95], 0\n"
    "v_mfma_f32_16x16x32_bf16 v[196:199], a[4:7], v[200:203], v[196:199]\n"
    "v_mfma_f32_16x16x32_bf16 v[196:199], a[8:11], v[32:35], v[196:199]\n"
    "v_mfma_f32_16x16x32_bf16 v[196:199], a[12:15], v[4:7], v[196:199]\n"
    "v_mfma_f32_16x16x32_bf16 v[196:199], a[16:19], v[96:99], v[196:199]\n"
    "v_mfma_f32_16x16x32_bf16 v[196:199], a[20:23], v[8:11], v[196:199]\n"
    "v_mfma_f32_16x16x32_bf16 v[136:139], a[24:27], v[92:95], 0\n"
    "v_cvt_pk_bf16_f32 v132, v132, v133\n"
    "v_cvt_pk_bf16_f32 v133, v134, v135\n"
    "v_mfma_f32_16x16x32_bf16 v[136:139], a[28:31], v[200:203], v[136:139]\n"
    "v_mfma_f32_16x16x32_bf16 v[136:139], a[32:35], v[32:35], v[136:139]\n"
    "v_mfma_f32_16x16x32_bf16 v[136:139], a[36:39], v[4:7], v[136:139]\n"
    "v_mfma_f32_16x16x32_bf16 v[136:139], a[40:43], v[96:99], v[136:139]\n"
    "v_mfma_f32_16x16x32_bf16 v[136:139], a[44:47], v[8:11], v[136:139]\n"
    "v_cvt_pk_bf16_f32 v48, v196, v197\n"
    "v_cvt_pk_bf16_f32 v49, v198, v199\n"
    "v_mfma_f32_16x16x32_bf16 v[0:3], a[48:51], v[92:95], 0\n"
    "v_mfma_f32_16x16x32_bf16 v[0:3], a[52:55], v[200:203], v[0:3]\n"
    "v_mfma_f32_16x16x32_bf16 v[0:3], a[56:59], v[32:35], v[0:3]\n"
    "v_mfma_f32_16x16x32_bf16 v[0:3], a[60:63], v[4:7], v[0:3]\n"
    "v_mfma_f32_16x16x32_bf16 v[0:3], a[64:67], v[96:99], v[0:3]\n"
    "v_mfma_f32_16x16x32_bf16 v[0:3], a[68:71], v[8:11], v[0:3]\n"
    "v_cvt_pk_bf16_f32 v50, v136, v137\n"
    "v_cvt_pk_bf16_f32 v51, v138, v139\n"
    "v_mfma_f32_16x16x32_bf16 v[12:15], a[72:75], v[92:95], 0\n"
    "v_mfma_f32_16x16x32_bf16 v[12:15], a[76:79], v[200:203], v[12:15]\n"
    "v_mfma_f32_16x16x32_bf16 v[12:15], a[80:83], v[32:35], v[12:15]\n"
    "v_mfma_f32_16x16x32_bf16 v[12:15], a[84:87], v[4:7], v[12:15]\n"
    "v_mfma_f32_16x16x32_bf16 v[12:15], a[88:91], v[96:99], v[12:15]\n"
    "v_mfma_f32_16x16x32_bf16 v[12:15], a[92:95], v[8:11], v[12:15]\n"
    "v_cvt_pk_bf16_f32 v62, v0, v1\n"
    "v_cvt_pk_bf16_f32 v63, v2, v3\n"
    "v_mfma_f32_16x16x32_bf16 v[156:159], a[96:99], v[92:95], 0\n"
    "v_mfma_f32_16x16x32_bf16 v[156:159], a[100:103], v[200:203], v[156:159]\n"
    "v_mfma_f32_16x16x32_bf16 v[156:159], a[104:107], v[32:35], v[156:159]\n"
    "v_mfma_f32_16x16x32_bf16 v[156:159], a[108:111], v[4:7], v[156:159]\n"
    "v_mfma_f32_16x16x32_bf16 v[156:159], a[112:115], v[96:99], v[156:159]\n"
    "v_mfma_f32_16x16x32_bf16 v[156:159], a[116:119], v[8:11], v[156:159]\n"
    "v_cvt_pk_bf16_f32 v76, v12, v13\n"
    "v_cvt_pk_bf16_f32 v77, v14, v15\n"
    "v_mfma_f32_16x16x32_bf16 v[88:91], a[120:123], v[92:95], 0\n"
    "v_mfma_f32_16x16x32_bf16 v[88:91], a[124:127], v[200:203], v[88:91]\n"
    "v_mfma_f32_16x16x32_bf16 v[88:91], a[128:131], v[32:35], v[88:91]\n"
    "v_mfma_f32_16x16x32_bf16 v[88:91], a[132:135], v[4:7], v[88:91]\n"
    "v_mfma_f32_16x16x32_bf16 v[88:91], a[136:139], v[96:99], v[88:91]\n"
    "v_mfma_f32_16x16x32_bf16 v[88:91], a[140:143], v[8:11], v[88:91]\n"
    "s_waitcnt vmcnt(6) lgkmcnt(0)\n"
    "v_cvt_pk_bf16_f32 v78, v156, v157\n"
    "v_cvt_pk_bf16_f32 v79, v158, v159\n"
    "s_barrier\n"
    "v_mfma_f32_16x16x32_bf16 v[0:3], v[92:95], a[144:147], 0\n"
    "ds_read_b128 v[136:139], v209 offset:0\n"
    "v_mfma_f32_16x16x32_bf16 v[0:3], v[200:203], a[148:151], v[0:3]\n"
    "ds_read_b128 v[140:143], v210 offset:1024\n"
    "v_mfma_f32_16x16x32_bf16 v[0:3], v[32:35], a[152:155], v[0:3]\n"
    "v_cvt_pk_bf16_f32 v86, v88, v89\n"
    "v_cvt_pk_bf16_f32 v87, v90, v91\n"
    "v_mfma_f32_16x16x32_bf16 v[0:3], v[4:7], a[156:159], v[0:3]\n"
    "ds_read_b128 v[144:147], v209 offset:2048\n"
    "v_mfma_f32_16x16x32_bf16 v[0:3], v[96:99], a[160:163], v[0:3]\n"
    "ds_read_b128 v[148:151], v210 offset:3072\n"
    "v_mfma_f32_16x16x32_bf16 v[0:3], v[8:11], a[164:167], v[0:3]\n"
    "ds_read_b128 v[170:173], v205 offset:24576\n"
    "v_mfma_f32_16x16x32_bf16 v[12:15], v[92:95], a[168:171], 0\n"
    "ds_read_b128 v[176:179], v206 offset:24576\n"
    "v_mfma_f32_16x16x32_bf16 v[12:15], v[200:203], a[172:175], v[12:15]\n"
    "ds_read_b128 v[152:155], v209 offset:4096\n"
    "v_mfma_f32_16x16x32_bf16 v[12:15], v[32:35], a[176:179], v[12:15]\n"
    "ds_read_b128 v[156:159], v210 offset:5120\n"
    "v_mfma_f32_16x16x32_bf16 v[12:15], v[4:7], a[180:183], v[12:15]\n"
    "v_cvt_pk_bf16_f32 v118, v0, v1\n"
    "v_cvt_pk_bf16_f32 v119, v2, v3\n"
    "v_mfma_f32_16x16x32_bf16 v[12:15], v[96:99], a[184:187], v[12:15]\n"
    "ds_read_b128 v[160:163], v209 offset:6144\n"
    "v_mfma_f32_16x16x32_bf16 v[12:15], v[8:11], a[188:191], v[12:15]\n"
    "ds_read_b128 v[164:167], v210 offset:7168\n"
    "v_mfma_f32_16x16x32_bf16 v[196:199], v[92:95], a[192:195], 0\n"
    "ds_read_b128 v[184:187], v205 offset:26624\n"
    "v_mfma_f32_16x16x32_bf16 v[196:199], v[200:203], a[196:199], v[196:199]\n"
    "ds_read_b128 v[190:193], v206 offset:26624\n"
    "v_mfma_f32_16x16x32_bf16 v[196:199], v[32:35], a[200:203], v[196:199]\n"
    "v_mfma_f32_16x16x32_bf16 v[196:199], v[4:7], a[204:207], v[196:199]\n"
    "v_mfma_f32_16x16x32_bf16 v[196:199], v[96:99], a[208:211], v[196:199]\n"
    "v_cvt_pk_bf16_f32 v126, v12, v13\n"
    "v_cvt_pk_bf16_f32 v127, v14, v15\n"
    "v_mfma_f32_16x16x32_bf16 v[196:199], v[8:11], a[212:215], v[196:199]\n"
    "s_nop 1\n"
    "s_waitcnt lgkmcnt(0)\n"
    "v_mfma_f32_16x16x32_bf16 v[136:139], v[36:39], v[64:67], v[136:139]\n"
    "ds_read_b128 v[0:3], v209 offset:8192\n"
    "v_mov_b64 v[4:5], v[84:85]\n"
    "v_mfma_f32_16x16x32_bf16 v[136:139], v[52:55], v[168:171], v[136:139]\n"
    "ds_read_b128 v[18:21], v210 offset:9216\n"
    "v_mov_b64 v[10:11], v[84:85]\n"
    "v_mfma_f32_16x16x32_bf16 v[140:143], v[40:43], v[64:67], v[140:143]\n"
    "ds_read_b128 v[22:25], v209 offset:10240\n"
    "v_mov_b64 v[16:17], v[84:85]\n"
    "v_mfma_f32_16x16x32_bf16 v[140:143], v[54:57], v[172:175], v[140:143]\n"
    "s_mov_b32 m0, s73\n"
    "ds_read_b128 v[26:29], v210 offset:11264\n"
    "v_mfma_f32_16x16x32_bf16 v[144:147], v[44:47], v[64:67], v[144:147]\n"
    "buffer_load_dwordx4 v218, s[40:43], s98 offen offset:0 nt lds\n"
    "v_mfma_f32_16x16x32_bf16 v[144:147], v[58:61], v[174:177], v[144:147]\n"
    "ds_read_b128 v[6:9], v205 offset:28672\n"
    "v_max3_f32 v30, v136, v137, v138\n"
    "v_mfma_f32_16x16x32_bf16 v[148:151], v[48:51], v[64:67], v[148:151]\n"
    "ds_read_b128 v[12:15], v206 offset:28672\n"
    "v_max3_f32 v31, v139, v140, v141\n"
    "v_mfma_f32_16x16x32_bf16 v[148:151], v[60:63], v[178:181], v[148:151]\n"
    "ds_read_b128 v[32:35], v209 offset:12288\n"
    "v_max3_f32 v30, v30, v142, v143\n"
    "v_mfma_f32_16x16x32_bf16 v[152:155], v[36:39], v[68:71], v[152:155]\n"
    "ds_read_b128 v[92:95], v210 offset:13312\n"
    "v_max3_f32 v31, v31, v144, v145\n"
    "v_mfma_f32_16x16x32_bf16 v[152:155], v[52:55], v[182:185], v[152:155]\n"
    "ds_read_b128 v[100:103], v209 offset:14336\n"
    "v_max3_f32 v30, v30, v146, v147\n"
    "v_mfma_f32_16x16x32_bf16 v[156:159], v[40:43], v[68:71], v[156:159]\n"
    "ds_read_b128 v[108:111], v210 offset:15360\n"
    "v_max3_f32 v31, v31, v148, v149\n"
    "v_mfma_f32_16x16x32_bf16 v[156:159], v[54:57], v[186:189], v[156:159]\n"
    "buffer_load_dwordx4 v219, s[40:43], s98 offen offset:1024 nt lds\n"
    "v_mfma_f32_16x16x32_bf16 v[160:163], v[44:47], v[68:71], v[160:163]\n"
    "ds_read_b128 v[170:173], v205 offset:30720\n"
    "v_max3_f32 v64, v152, v153, v154\n"
    "v_mfma_f32_16x16x32_bf16 v[160:163], v[58:61], v[188:191], v[160:163]\n"
    "ds_read_b128 v[176:179], v206 offset:30720\n"
    "v_max3_f32 v30, v30, v150, v151\n"
    "v_mfma_f32_16x16x32_bf16 v[164:167], v[48:51], v[68:71], v[164:167]\n"
    "ds_read_b128 v[66:69], v207 offset:0\n"
    "v_max3_f32 v65, v155, v156, v157\n"
    "v_mfma_f32_16x16x32_bf16 v[164:167], v[60:63], v[192:195], v[164:167]\n"
    "ds_read_b128 v[88:91], v208 offset:0\n"
    "v_max3_f32 v64, v64, v158, v159\n"
    "s_waitcnt lgkmcnt(10)\n"
    "v_mfma_f32_16x16x32_bf16 v[0:3], v[36:39], v[72:75], v[0:3]\n"
    "ds_read_b128 v[96:99], v207 offset:8192\n"
    "v_max3_f32 v65, v65, v160, v161\n"
    "s_waitcnt lgkmcnt(10)\n"
    "v_mfma_f32_16x16x32_bf16 v[0:3], v[52:55], v[4:7], v[0:3]\n"
    "ds_read_b128 v[4:7], v208 offset:8192\n"
    "v_max3_f32 v64, v64, v162, v163\n"
    "v_mfma_f32_16x16x32_bf16 v[18:21], v[40:43], v[72:75], v[18:21]\n"
    "ds_read_b128 v[104:107], v207 offset:16384\n"
    "v_max3_f32 v65, v65, v164, v165\n"
    "v_mfma_f32_16x16x32_bf16 v[18:21], v[54:57], v[8:11], v[18:21]\n"
    "ds_read_b128 v[182:185], v208 offset:16384\n"
    "v_max3_f32 v64, v64, v166, v167\n"
    "v_mfma_f32_16x16x32_bf16 v[22:25], v[44:47], v[72:75], v[22:25]\n"
    "s_waitcnt lgkmcnt(10)\n"
    "v_max_f32 v30, v30, v31\n"
    "v_max_f32 v64, v64, v65\n"
    "v_mfma_f32_16x16x32_bf16 v[22:25], v[58:61], v[10:13], v[22:25]\n"
    "v_mov_b64 v[168:169], v[86:87]\n"
    "v_mov_b64 v[174:175], v[86:87]\n"
    "v_mfma_f32_16x16x32_bf16 v[26:29], v[48:51], v[72:75], v[26:29]\n"
    "v_permlane32_swap_b32 v30, v64\n"
    "v_mfma_f32_16x16x32_bf16 v[26:29], v[60:63], v[14:17], v[26:29]\n"
    "s_xor_b32 s73, s73, 0x8000\n"
    "v_max_f32 v30, v30, v64\n"
    "v_mov_b64 v[180:181], v[86:87]\n"
    "v_mfma_f32_16x16x32_bf16 v[32:35], v[36:39], v[76:79], v[32:35]\n"
    "s_waitcnt lgkmcnt(7)\n"
    "v_mov_b32 v64, v30\n"
    "v_xor_b32 v205, 0x8000, v205\n"
    "v_mfma_f32_16x16x32_bf16 v[32:35], v[52:55], v[168:171], v[32:35]\n"
    "v_permlane16_swap_b32 v30, v64\n"
    "v_mfma_f32_16x16x32_bf16 v[92:95], v[40:43], v[76:79], v[92:95]\n"
    "v_max_f32 v30, v30, v64\n"
    "v_xor_b32 v206, 0x8000, v206\n"
    "v_mfma_f32_16x16x32_bf16 v[92:95], v[54:57], v[172:175], v[92:95]\n"
    "v_mul_f32 v30, s71, v30\n"
    "v_cvt_pk_bf16_f32 v134, v196, v197\n"
    "v_mfma_f32_16x16x32_bf16 v[100:103], v[44:47], v[76:79], v[100:103]\n"
    "s_waitcnt lgkmcnt(3)\n"
    "v_mov_b32 v64, v30\n"
    "v_cvt_pk_bf16_f32 v135, v198, v199\n"
    "v_mfma_f32_16x16x32_bf16 v[100:103], v[58:61], v[174:177], v[100:103]\n"
    "ds_read_b128 v[8:11], v207 offset:2048\n"
    "v_max3_f32 v12, v0, v1, v2\n"
    "v_mfma_f32_16x16x32_bf16 v[108:111], v[48:51], v[76:79], v[108:111]\n"
    "ds_read_b128 v[14:17], v208 offset:2048\n"
    "v_max3_f32 v13, v32, v33, v34\n"
    "v_mfma_f32_16x16x32_bf16 v[108:111], v[60:63], v[178:181], v[108:111]\n"
    "ds_read_b128 v[36:39], v207 offset:10240\n"
    "v_max3_f32 v31, v3, v18, v19\n"
    "v_mfma_f32_16x16x32_bf16 v[40:43], a[216:219], v[66:69], 0\n"
    "ds_read_b128 v[44:47], v208 offset:10240\n"
    "v_max3_f32 v12, v12, v20, v21\n"
    "v_mfma_f32_16x16x32_bf16 v[40:43], a[220:223], v[88:91], v[40:43]\n"
    "ds_read_b128 v[48:51], v207 offset:18432\n"
    "v_max3_f32 v52, v35, v92, v93\n"
    "v_mfma_f32_16x16x32_bf16 v[40:43], a[224:227], v[96:99], v[40:43]\n"
    "ds_read_b128 v[56:59], v208 offset:18432\n"
    "v_max3_f32 v13, v13, v94, v95\n"
    "s_waitcnt lgkmcnt(6)\n"
    "v_mfma_f32_16x16x32_bf16 v[40:43], a[228:231], v[4:7], v[40:43]\n"
    "v_permlane32_swap_b32 v30, v64\n"
    "v_mfma_f32_16x16x32_bf16 v[40:43], a[232:235], v[104:107], v[40:43]\n"
    "v_fma_f32 v136, v136, s68, v30\n"
    "v_fma_f32 v137, v137, s68, v30\n"
    "v_mfma_f32_16x16x32_bf16 v[40:43], a[236:239], v[182:185], v[40:43]\n"
    "v_fma_f32 v138, v138, s68, v30\n"
    "v_fma_f32 v139, v139, s68, v30\n"
    "v_mfma_f32_16x16x32_bf16 v[70:73], a[240:243], v[66:69], 0\n"
    "v_fma_f32 v140, v140, s68, v30\n"
    "v_fma_f32 v141, v141, s68, v30\n"
    "v_mfma_f32_16x16x32_bf16 v[70:73], a[244:247], v[88:91], v[70:73]\n"
    "v_fma_f32 v142, v142, s68, v30\n"
    "v_fma_f32 v143, v143, s68, v30\n"
    "v_mfma_f32_16x16x32_bf16 v[70:73], a[248:251], v[96:99], v[70:73]\n"
    "v_cvt_pk_bf16_f32 v40, v40, v41\n"
    "v_cvt_pk_bf16_f32 v41, v42, v43\n"
    "v_mfma_f32_16x16x32_bf16 v[70:73], a[252:255], v[4:7], v[70:73]\n"
    "v_exp_f32 v136, v136\n"
    "v_mfma_f32_16x16x32_bf16 v[70:73], v[224:227], v[104:107], v[70:73]\n"
    "v_exp_f32 v137, v137\n"
    "v_mfma_f32_16x16x32_bf16 v[70:73], v[228:231], v[182:185], v[70:73]\n"
    "v_exp_f32 v138, v138\n"
    "v_mfma_f32_16x16x32_bf16 v[74:77], v[232:235], v[66:69], 0\n"
    "v_exp_f32 v139, v139\n"
    "v_mfma_f32_16x16x32_bf16 v[74:77], v[236:239], v[88:91], v[74:77]\n"
    "v_exp_f32 v140, v140\n"
    "v_mfma_f32_16x16x32_bf16 v[74:77], v[240:243], v[96:99], v[74:77]\n"
    "v_exp_f32 v141, v141\n"
    "v_mfma_f32_16x16x32_bf16 v[74:77], v[244:247], v[4:7], v[74:77]\n"
    "ds_read_b128 v[4:7], v207 offset:4096\n"
    "v_cvt_pk_bf16_f32 v42, v70, v71\n"
    "v_mfma_f32_16x16x32_bf16 v[74:77], v[248:251], v[104:107], v[74:77]\n"
    "ds_read_b128 v[66:69], v208 offset:4096\n"
    "v_cvt_pk_bf16_f32 v43, v72, v73\n"
    "v_mfma_f32_16x16x32_bf16 v[74:77], v[252:255], v[182:185], v[74:77]\n"
    "buffer_store_dwordx4 v[40:43], v214, s[44:47], s76 offen offset:0 nt\n"
    "s_waitcnt lgkmcnt(2)\n"
    "v_mfma_f32_16x16x32_bf16 v[70:73], a[216:219], v[8:11], 0\n"
    "ds_read_b128 v[84:87], v207 offset:12288\n"
    "v_fma_f32 v144, v144, s68, v30\n"
    "v_mfma_f32_16x16x32_bf16 v[70:73], a[220:223], v[14:17], v[70:73]\n"
    "ds_read_b128 v[40:43], v208 offset:12288\n"
    "v_fma_f32 v145, v145, s68, v30\n"
    "v_mfma_f32_16x16x32_bf16 v[70:73], a[224:227], v[36:39], v[70:73]\n"
    "ds_read_b128 v[88:91], v207 offset:20480\n"
    "v_cvt_pk_bf16_f32 v62, v74, v75\n"
    "v_mfma_f32_16x16x32_bf16 v[70:73], a[228:231], v[44:47], v[70:73]\n"
    "ds_read_b128 v[96:99], v208 offset:20480\n"
    "v_cvt_pk_bf16_f32 v63, v76, v77\n"
    "v_mfma_f32_16x16x32_bf16 v[70:73], a[232:235], v[48:51], v[70:73]\n"
    "buffer_store_dwordx2 v[62:63], v215, s[44:47], s76 offen offset:0 nt\n"
    "v_mfma_f32_16x16x32_bf16 v[70:73], a[236:239], v[56:59], v[70:73]\n"
    "v_exp_f32 v142, v142\n"
    "v_mfma_f32_16x16x32_bf16 v[74:77], a[240:243], v[8:11], 0\n"
    "v_exp_f32 v143, v143\n"
    "v_mfma_f32_16x16x32_bf16 v[74:77], a[244:247], v[14:17], v[74:77]\n"
    "v_fma_f32 v146, v146, s68, v30\n"
    "v_fma_f32 v147, v147, s68, v30\n"
    "v_mfma_f32_16x16x32_bf16 v[74:77], a[248:251], v[36:39], v[74:77]\n"
    "v_fma_f32 v148, v148, s68, v30\n"
    "v_cvt_pk_bf16_f32 v70, v70, v71\n"
    "v_mfma_f32_16x16x32_bf16 v[74:77], a[252:255], v[44:47], v[74:77]\n"
    "v_cvt_pk_bf16_f32 v71, v72, v73\n"
    "v_fma_f32 v149, v149, s68, v30\n"
    "v_mfma_f32_16x16x32_bf16 v[74:77], v[224:227], v[48:51], v[74:77]\n"
    "v_fma_f32 v150, v150, s68, v30\n"
    "v_fma_f32 v151, v151, s68, v30\n"
    "v_mfma_f32_16x16x32_bf16 v[74:77], v[228:231], v[56:59], v[74:77]\n"
    "ds_read_b128 v[104:107], v207 offset:6144\n"
    "v_cvt_pk_bf16_f32 v136, v136, v137\n"
    "v_mfma_f32_16x16x32_bf16 v[168:171], v[232:235], v[8:11], 0\n"
    "ds_read_b128 v[8:11], v208 offset:6144\n"
    "v_cvt_pk_bf16_f32 v137, v138, v139\n"
    "v_mfma_f32_16x16x32_bf16 v[168:171], v[236:239], v[14:17], v[168:171]\n"
    "ds_read_b128 v[14:17], v207 offset:14336\n"
    "v_cvt_pk_bf16_f32 v138, v140, v141\n"
    "v_mfma_f32_16x16x32_bf16 v[168:171], v[240:243], v[36:39], v[168:171]\n"
    "ds_read_b128 v[36:39], v208 offset:14336\n"
    "v_cvt_pk_bf16_f32 v72, v74, v75\n"
    "v_mfma_f32_16x16x32_bf16 v[168:171], v[244:247], v[44:47], v[168:171]\n"
    "ds_read_b128 v[44:47], v207 offset:22528\n"
    "v_cvt_pk_bf16_f32 v73, v76, v77\n"
    "v_mfma_f32_16x16x32_bf16 v[168:171], v[248:251], v[48:51], v[168:171]\n"
    "buffer_store_dwordx4 v[70:73], v214, s[44:47], s77 offen offset:0 nt\n"
    "v_mfma_f32_16x16x32_bf16 v[168:171], v[252:255], v[56:59], v[168:171]\n"
    "ds_read_b128 v[48:51], v208 offset:22528\n"
    "v_cvt_pk_bf16_f32 v139, v142, v143\n"
    "s_waitcnt lgkmcnt(6)\n"
    "v_mfma_f32_16x16x32_bf16 v[56:59], a[216:219], v[4:7], 0\n"
    "v_exp_f32 v144, v144\n"
    "v_mfma_f32_16x16x32_bf16 v[70:73], v[220:223], v[136:139], 0\n"
    "v_exp_f32 v145, v145\n"
    "v_mfma_f32_16x16x32_bf16 v[56:59], a[220:223], v[66:69], v[56:59]\n"
    "v_exp_f32 v146, v146\n"
    "v_mfma_f32_16x16x32_bf16 v[56:59], a[224:227], v[84:87], v[56:59]\n"
    "v_cvt_pk_bf16_f32 v62, v168, v169\n"
    "v_cvt_pk_bf16_f32 v63, v170, v171\n"
    "v_mfma_f32_16x16x32_bf16 v[56:59], a[228:231], v[40:43], v[56:59]\n"
    "buffer_store_dwordx2 v[62:63], v215, s[44:47], s77 offen offset:0 nt\n"
    "v_mfma_f32_16x16x32_bf16 v[56:59], a[232:235], v[88:91], v[56:59]\n"
    "v_exp_f32 v147, v147\n"
    "v_mfma_f32_16x16x32_bf16 v[56:59], a[236:239], v[96:99], v[56:59]\n"
    "v_exp_f32 v148, v148\n"
    "v_mfma_f32_16x16x32_bf16 v[74:77], a[240:243], v[4:7], 0\n"
    "v_exp_f32 v149, v149\n"
    "v_mfma_f32_16x16x32_bf16 v[74:77], a[244:247], v[66:69], v[74:77]\n"
    "v_exp_f32 v150, v150\n"
    "v_mfma_f32_16x16x32_bf16 v[74:77], a[248:251], v[84:87], v[74:77]\n"
    "v_exp_f32 v151, v151\n"
    "v_mfma_f32_16x16x32_bf16 v[74:77], a[252:255], v[40:43], v[74:77]\n"
    "v_cvt_pk_bf16_f32 v56, v56, v57\n"
    "v_cvt_pk_bf16_f32 v57, v58, v59\n"
    "v_mfma_f32_16x16x32_bf16 v[74:77], v[224:227], v[88:91], v[74:77]\n"
    "v_cvt_pk_bf16_f32 v140, v144, v145\n"
    "v_cvt_pk_bf16_f32 v141, v146, v147\n"
    "v_mfma_f32_16x16x32_bf16 v[74:77], v[228:231], v[96:99], v[74:77]\n"
    "v_cvt_pk_bf16_f32 v142, v148, v149\n"
    "v_cvt_pk_bf16_f32 v143, v150, v151\n"
    "v_mfma_f32_16x16x32_bf16 v[144:147], v[232:235], v[4:7], 0\n"
    "v_max3_f32 v31, v31, v22, v23\n"
    "v_max3_f32 v12, v12, v24, v25\n"
    "v_mfma_f32_16x16x32_bf16 v[70:73], v[220:223], v[140:143], v[70:73]\n"
    "v_max3_f32 v52, v52, v100, v101\n"
    "v_max3_f32 v13, v13, v102, v103\n"
    "v_mfma_f32_16x16x32_bf16 v[4:7], v[112:115], v[136:139], 0\n"
    "v_cvt_pk_bf16_f32 v58, v74, v75\n"
    "v_cvt_pk_bf16_f32 v59, v76, v77\n"
    "v_mfma_f32_16x16x32_bf16 v[4:7], v[116:119], v[140:143], v[4:7]\n"
    "buffer_store_dwordx4 v[56:59], v214, s[44:47], s78 offen offset:0 nt\n"
    "v_mfma_f32_16x16x32_bf16 v[74:77], v[120:123], v[136:139], 0\n"
    "v_rcp_f32 v30, v70\n"
    "v_mfma_f32_16x16x32_bf16 v[74:77], v[124:127], v[140:143], v[74:77]\n"
    "v_max3_f32 v31, v31, v26, v27\n"
    "v_max3_f32 v12, v12, v28, v29\n"
    "v_mfma_f32_16x16x32_bf16 v[56:59], v[128:131], v[136:139], 0\n"
    "v_max3_f32 v52, v52, v108, v109\n"
    "v_max3_f32 v13, v13, v110, v111\n"
    "v_mfma_f32_16x16x32_bf16 v[56:59], v[132:135], v[140:143], v[56:59]\n"
    "v_mul_f32 v4, v4, v30\n"
    "v_mul_f32 v5, v5, v30\n"
    "v_mfma_f32_16x16x32_bf16 v[144:147], v[236:239], v[66:69], v[144:147]\n"
    "v_mul_f32 v6, v6, v30\n"
    "v_mul_f32 v7, v7, v30\n"
    "v_mfma_f32_16x16x32_bf16 v[144:147], v[240:243], v[84:87], v[144:147]\n"
    "v_mul_f32 v74, v74, v30\n"
    "v_mul_f32 v75, v75, v30\n"
    "v_mfma_f32_16x16x32_bf16 v[144:147], v[244:247], v[40:43], v[144:147]\n"
    "v_mul_f32 v76, v76, v30\n"
    "v_mul_f32 v77, v77, v30\n"
    "v_mfma_f32_16x16x32_bf16 v[144:147], v[248:251], v[88:91], v[144:147]\n"
    "v_mul_f32 v56, v56, v30\n"
    "v_mul_f32 v57, v57, v30\n"
    "v_mfma_f32_16x16x32_bf16 v[144:147], v[252:255], v[96:99], v[144:147]\n"
    "s_waitcnt lgkmcnt(0)\n"
    "v_mul_f32 v58, v58, v30\n"
    "v_mul_f32 v59, v59, v30\n"
    "v_mfma_f32_16x16x32_bf16 v[40:43], a[216:219], v[104:107], 0\n"
    "v_max_f32 v12, v12, v31\n"
    "v_max_f32 v13, v13, v52\n"
    "v_mfma_f32_16x16x32_bf16 v[40:43], a[220:223], v[8:11], v[40:43]\n"
    "v_cvt_pk_bf16_f32 v4, v4, v5\n"
    "v_cvt_pk_bf16_f32 v5, v6, v7\n"
    "v_mfma_f32_16x16x32_bf16 v[40:43], a[224:227], v[14:17], v[40:43]\n"
    "v_cvt_pk_bf16_f32 v6, v144, v145\n"
    "v_cvt_pk_bf16_f32 v7, v146, v147\n"
    "v_mfma_f32_16x16x32_bf16 v[40:43], a[228:231], v[36:39], v[40:43]\n"
    "buffer_store_dwordx2 v[6:7], v215, s[44:47], s78 offen offset:0 nt\n"
    "v_mfma_f32_16x16x32_bf16 v[40:43], a[232:235], v[44:47], v[40:43]\n"
    "v_cvt_pk_bf16_f32 v30, v74, v75\n"
    "v_cvt_pk_bf16_f32 v31, v76, v77\n"
    "v_mfma_f32_16x16x32_bf16 v[40:43], a[236:239], v[48:51], v[40:43]\n"
    "s_waitcnt vmcnt(8)\n"
    "s_barrier\n"
    "v_mfma_f32_16x16x32_bf16 v[66:69], a[240:243], v[104:107], 0\n"
    "ds_write_b64 v211, v[4:5] offset:0\n"
    "v_mfma_f32_16x16x32_bf16 v[66:69], a[244:247], v[8:11], v[66:69]\n"
    "ds_read_b128 v[4:7], v205 offset:0\n"
    "v_cvt_pk_bf16_f32 v52, v56, v57\n"
    "v_mfma_f32_16x16x32_bf16 v[66:69], a[248:251], v[14:17], v[66:69]\n"
    "ds_read_b128 v[70:73], v206 offset:0\n"
    "v_cvt_pk_bf16_f32 v40, v40, v41\n"
    "v_mfma_f32_16x16x32_bf16 v[66:69], a[252:255], v[36:39], v[66:69]\n"
    "ds_write_b64 v212, v[30:31] offset:0\n"
    "v_mfma_f32_16x16x32_bf16 v[66:69], v[224:227], v[44:47], v[66:69]\n"
    "ds_read_b128 v[74:77], v205 offset:8192\n"
    "v_cvt_pk_bf16_f32 v41, v42, v43\n"
    "v_mfma_f32_16x16x32_bf16 v[66:69], v[228:231], v[48:51], v[66:69]\n"
    "ds_read_b128 v[84:87], v206 offset:8192\n"
    "v_cvt_pk_bf16_f32 v53, v58, v59\n"
    "v_mfma_f32_16x16x32_bf16 v[56:59], v[232:235], v[104:107], 0\n"
    "ds_write_b64 v213, v[52:53] offset:0\n"
    "v_mfma_f32_16x16x32_bf16 v[56:59], v[236:239], v[8:11], v[56:59]\n"
    "ds_read_b128 v[8:11], v205 offset:16384\n"
    "v_fma_f32 v152, v152, s68, v64\n"
    "v_mfma_f32_16x16x32_bf16 v[56:59], v[240:243], v[14:17], v[56:59]\n"
    "ds_read_b128 v[14:17], v206 offset:16384\n"
    "v_cvt_pk_bf16_f32 v42, v66, v67\n"
    "v_mfma_f32_16x16x32_bf16 v[56:59], v[244:247], v[36:39], v[56:59]\n"
    "v_cvt_pk_bf16_f32 v43, v68, v69\n"
    "v_fma_f32 v153, v153, s68, v64\n"
    "v_mfma_f32_16x16x32_bf16 v[56:59], v[248:251], v[44:47], v[56:59]\n"
    "buffer_store_dwordx4 v[40:43], v214, s[44:47], s79 offen offset:0 nt\n"
    "v_mfma_f32_16x16x32_bf16 v[56:59], v[252:255], v[48:51], v[56:59]\n"
    "s_waitcnt lgkmcnt(7)\n"
    "v_permlane32_swap_b32 v12, v13\n"
    "v_mfma_f32_16x16x32_bf16 v[36:39], a[0:3], v[4:7], 0\n"
    "s_waitcnt lgkmcnt(6)\n"
    "v_max_f32 v12, v12, v13\n"
    "v_fma_f32 v154, v154, s68, v64\n"
    "v_mfma_f32_16x16x32_bf16 v[36:39], a[4:7], v[70:73], v[36:39]\n"
    "s_waitcnt lgkmcnt(4)\n"
    "v_mov_b32 v13, v12\n"
    "v_fma_f32 v155, v155, s68, v64\n"
    "v_mfma_f32_16x16x32_bf16 v[36:39], a[8:11], v[74:77], v[36:39]\n"
    "s_waitcnt lgkmcnt(3)\n"
    "v_cvt_pk_bf16_f32 v30, v56, v57\n"
    "v_cvt_pk_bf16_f32 v31, v58, v59\n"
    "v_mfma_f32_16x16x32_bf16 v[36:39], a[12:15], v[84:87], v[36:39]\n"
    "buffer_store_dwordx2 v[30:31], v215, s[44:47], s79 offen offset:0 nt\n"
    "s_waitcnt lgkmcnt(1)\n"
    "v_mfma_f32_16x16x32_bf16 v[36:39], a[16:19], v[8:11], v[36:39]\n"
    "s_waitcnt lgkmcnt(0)\n"
    "v_fma_f32 v156, v156, s68, v64\n"
    "v_fma_f32 v157, v157, s68, v64\n"
    "v_mfma_f32_16x16x32_bf16 v[36:39], a[20:23], v[14:17], v[36:39]\n"
    "v_fma_f32 v158, v158, s68, v64\n"
    "v_fma_f32 v159, v159, s68, v64\n"
    "v_mfma_f32_16x16x32_bf16 v[40:43], a[24:27], v[4:7], 0\n"
    "v_exp_f32 v152, v152\n"
    "v_mfma_f32_16x16x32_bf16 v[40:43], a[28:31], v[70:73], v[40:43]\n"
    "v_exp_f32 v153, v153\n"
    "v_mfma_f32_16x16x32_bf16 v[40:43], a[32:35], v[74:77], v[40:43]\n"
    "v_exp_f32 v154, v154\n"
    "v_mfma_f32_16x16x32_bf16 v[40:43], a[36:39], v[84:87], v[40:43]\n"
    "v_cvt_pk_bf16_f32 v36, v36, v37\n"
    "v_cvt_pk_bf16_f32 v37, v38, v39\n"
    "v_mfma_f32_16x16x32_bf16 v[40:43], a[40:43], v[8:11], v[40:43]\n"
    "v_exp_f32 v155, v155\n"
    "v_mfma_f32_16x16x32_bf16 v[40:43], a[44:47], v[14:17], v[40:43]\n"
    "v_exp_f32 v156, v156\n"
    "v_mfma_f32_16x16x32_bf16 v[44:47], a[48:51], v[4:7], 0\n"
    "v_exp_f32 v157, v157\n"
    "v_mfma_f32_16x16x32_bf16 v[44:47], a[52:55], v[70:73], v[44:47]\n"
    "v_exp_f32 v158, v158\n"
    "v_mfma_f32_16x16x32_bf16 v[44:47], a[56:59], v[74:77], v[44:47]\n"
    "v_exp_f32 v159, v159\n"
    "v_mfma_f32_16x16x32_bf16 v[44:47], a[60:63], v[84:87], v[44:47]\n"
    "v_cvt_pk_bf16_f32 v38, v40, v41\n"
    "v_cvt_pk_bf16_f32 v39, v42, v43\n"
    "v_mfma_f32_16x16x32_bf16 v[44:47], a[64:67], v[8:11], v[44:47]\n"
    "v_fma_f32 v160, v160, s68, v64\n"
    "v_fma_f32 v161, v161, s68, v64\n"
    "v_mfma_f32_16x16x32_bf16 v[44:47], a[68:71], v[14:17], v[44:47]\n"
    "v_fma_f32 v162, v162, s68, v64\n"
    "v_fma_f32 v163, v163, s68, v64\n"
    "v_mfma_f32_16x16x32_bf16 v[40:43], a[72:75], v[4:7], 0\n"
    "v_fma_f32 v164, v164, s68, v64\n"
    "v_fma_f32 v165, v165, s68, v64\n"
    "v_mfma_f32_16x16x32_bf16 v[40:43], a[76:79], v[70:73], v[40:43]\n"
    "v_fma_f32 v166, v166, s68, v64\n"
    "v_fma_f32 v167, v167, s68, v64\n"
    "v_mfma_f32_16x16x32_bf16 v[40:43], a[80:83], v[74:77], v[40:43]\n"
    "v_cvt_pk_bf16_f32 v52, v44, v45\n"
    "v_cvt_pk_bf16_f32 v53, v46, v47\n"
    "v_mfma_f32_16x16x32_bf16 v[40:43], a[84:87], v[84:87], v[40:43]\n"
    "v_cvt_pk_bf16_f32 v44, v152, v153\n"
    "v_cvt_pk_bf16_f32 v45, v154, v155\n"
    "v_mfma_f32_16x16x32_bf16 v[40:43], a[88:91], v[8:11], v[40:43]\n"
    "v_cvt_pk_bf16_f32 v46, v156, v157\n"
    "v_cvt_pk_bf16_f32 v47, v158, v159\n"
    "v_mfma_f32_16x16x32_bf16 v[40:43], a[92:95], v[14:17], v[40:43]\n"
    "v_exp_f32 v160, v160\n"
    "v_mfma_f32_16x16x32_bf16 v[48:51], v[220:223], v[44:47], 0\n"
    "v_exp_f32 v161, v161\n"
    "v_mfma_f32_16x16x32_bf16 v[56:59], a[96:99], v[4:7], 0\n"
    "v_exp_f32 v162, v162\n"
    "v_mfma_f32_16x16x32_bf16 v[56:59], a[100:103], v[70:73], v[56:59]\n"
    "v_exp_f32 v163, v163\n"
    "v_mfma_f32_16x16x32_bf16 v[56:59], a[104:107], v[74:77], v[56:59]\n"
    "v_cvt_pk_bf16_f32 v64, v40, v41\n"
    "v_cvt_pk_bf16_f32 v65, v42, v43\n"
    "v_mfma_f32_16x16x32_bf16 v[56:59], a[108:111], v[84:87], v[56:59]\n"
    "v_exp_f32 v164, v164\n"
    "v_mfma_f32_16x16x32_bf16 v[56:59], a[112:115], v[8:11], v[56:59]\n"
    "v_exp_f32 v165, v165\n"
    "v_mfma_f32_16x16x32_bf16 v[56:59], a[116:119], v[14:17], v[56:59]\n"
    "s_cmp_eq_u32 s33, 0\n"
    "s_cbranch_scc1 LNOX0%=\n"
    "s_add_u32 m0, s72, 0\n"
    "s_add_u32 s101, s95, 0\n"
    "buffer_load_dwordx4 v216, s[36:39], s101 offen offset:0 nt lds\n"
    "buffer_load_dwordx4 v217, s[36:39], s101 offen offset:1024 nt lds\n"
    "LNOX0%=:\n"
    "LPG1%=:\n"
    "s_cbranch_execz LWB%=\n"
    "v_cvt_pk_bf16_f32 v40, v160, v161\n"
    "v_mfma_f32_16x16x32_bf16 v[88:91], a[120:123], v[4:7], 0\n"
    "v_exp_f32 v166, v166\n"
    "v_mfma_f32_16x16x32_bf16 v[88:91], a[124:127], v[70:73], v[88:91]\n"
    "v_exp_f32 v167, v167\n"
    "v_mfma_f32_16x16x32_bf16 v[88:91], a[128:131], v[74:77], v[88:91]\n"
    "v_permlane16_swap_b32 v12, v13\n"
    "v_mfma_f32_16x16x32_bf16 v[88:91], a[132:135], v[84:87], v[88:91]\n"
    "ds_read_b128 v[136:139], v205 offset:2048\n"
    "v_cvt_pk_bf16_f32 v66, v56, v57\n"
    "v_mfma_f32_16x16x32_bf16 v[88:91], a[136:139], v[8:11], v[88:91]\n"
    "ds_read_b128 v[140:143], v206 offset:2048\n"
    "v_cvt_pk_bf16_f32 v67, v58, v59\n"
    "v_mfma_f32_16x16x32_bf16 v[88:91], a[140:143], v[14:17], v[88:91]\n"
    "ds_read_b128 v[144:147], v205 offset:10240\n"
    "v_cvt_pk_bf16_f32 v41, v162, v163\n"
    "v_mfma_f32_16x16x32_bf16 v[56:59], v[4:7], a[144:147], 0\n"
    "ds_read_b128 v[148:151], v206 offset:10240\n"
    "v_cvt_pk_bf16_f32 v42, v164, v165\n"
    "v_mfma_f32_16x16x32_bf16 v[56:59], v[70:73], a[148:151], v[56:59]\n"
    "ds_read_b128 v[152:155], v205 offset:18432\n"
    "v_cvt_pk_bf16_f32 v43, v166, v167\n"
    "v_mfma_f32_16x16x32_bf16 v[56:59], v[74:77], a[152:155], v[56:59]\n"
    "ds_read_b128 v[156:159], v206 offset:18432\n"
    "v_cvt_pk_bf16_f32 v80, v88, v89\n"
    "v_mfma_f32_16x16x32_bf16 v[48:51], v[220:223], v[40:43], v[48:51]\n"
    "v_cvt_pk_bf16_f32 v81, v90, v91\n"
    "v_max_f32 v12, v12, v13\n"
    "v_mfma_f32_16x16x32_bf16 v[88:91], v[112:115], v[44:47], 0\n"
    "v_mul_f32 v12, s71, v12\n"
    "v_mov_b64 v[168:169], v[80:81]\n"
    "v_mfma_f32_16x16x32_bf16 v[88:91], v[116:119], v[40:43], v[88:91]\n"
    "v_mov_b32 v13, v12\n"
    "v_mov_b64 v[174:175], v[80:81]\n"
    "v_mfma_f32_16x16x32_bf16 v[96:99], v[120:123], v[44:47], 0\n"
    "v_rcp_f32 v30, v48\n"
    "v_mfma_f32_16x16x32_bf16 v[96:99], v[124:127], v[40:43], v[96:99]\n"
    "v_permlane32_swap_b32 v12, v13\n"
    "v_mfma_f32_16x16x32_bf16 v[48:51], v[128:131], v[44:47], 0\n"
    "v_fma_f32 v0, v0, s68, v12\n"
    "v_mul_f32 v88, v88, v30\n"
    "v_mfma_f32_16x16x32_bf16 v[48:51], v[132:135], v[40:43], v[48:51]\n"
    "v_mul_f32 v89, v89, v30\n"
    "v_mul_f32 v90, v90, v30\n"
    "v_mfma_f32_16x16x32_bf16 v[56:59], v[84:87], a[156:159], v[56:59]\n"
    "v_mul_f32 v91, v91, v30\n"
    "v_mul_f32 v96, v96, v30\n"
    "v_mfma_f32_16x16x32_bf16 v[56:59], v[8:11], a[160:163], v[56:59]\n"
    "v_mul_f32 v97, v97, v30\n"
    "v_mul_f32 v98, v98, v30\n"
    "v_mfma_f32_16x16x32_bf16 v[56:59], v[14:17], a[164:167], v[56:59]\n"
    "v_mul_f32 v99, v99, v30\n"
    "v_mul_f32 v48, v48, v30\n"
    "v_mfma_f32_16x16x32_bf16 v[40:43], v[4:7], a[168:171], 0\n"
    "v_mul_f32 v49, v49, v30\n"
    "v_mul_f32 v50, v50, v30\n"
    "v_mfma_f32_16x16x32_bf16 v[40:43], v[70:73], a[172:175], v[40:43]\n"
    "v_mul_f32 v51, v51, v30\n"
    "v_cvt_pk_bf16_f32 v30, v88, v89\n"
    "v_mfma_f32_16x16x32_bf16 v[40:43], v[74:77], a[176:179], v[40:43]\n"
    "v_cvt_pk_bf16_f32 v88, v56, v57\n"
    "v_cvt_pk_bf16_f32 v89, v58, v59\n"
    "v_mfma_f32_16x16x32_bf16 v[40:43], v[84:87], a[180:183], v[40:43]\n"
    "v_cvt_pk_bf16_f32 v31, v90, v91\n"
    "v_cvt_pk_bf16_f32 v44, v96, v97\n"
    "v_mfma_f32_16x16x32_bf16 v[40:43], v[8:11], a[184:187], v[40:43]\n"
    "ds_write_b64 v211, v[30:31] offset:2048\n"
    "v_mfma_f32_16x16x32_bf16 v[40:43], v[14:17], a[188:191], v[40:43]\n"
    "v_cvt_pk_bf16_f32 v45, v98, v99\n"
    "v_cvt_pk_bf16_f32 v30, v48, v49\n"
    "v_mfma_f32_16x16x32_bf16 v[46:49], v[4:7], a[192:195], 0\n"
    "v_cvt_pk_bf16_f32 v31, v50, v51\n"
    "v_fma_f32 v1, v1, s68, v12\n"
    "v_mfma_f32_16x16x32_bf16 v[46:49], v[70:73], a[196:199], v[46:49]\n"
    "ds_write_b64 v212, v[44:45] offset:2048\n"
    "v_mfma_f32_16x16x32_bf16 v[46:49], v[74:77], a[200:203], v[46:49]\n"
    "v_cvt_pk_bf16_f32 v96, v40, v41\n"
    "v_cvt_pk_bf16_f32 v97, v42, v43\n"
    "v_mfma_f32_16x16x32_bf16 v[46:49], v[84:87], a[204:207], v[46:49]\n"
    "v_fma_f32 v2, v2, s68, v12\n"
    "v_fma_f32 v3, v3, s68, v12\n"
    "v_mfma_f32_16x16x32_bf16 v[46:49], v[8:11], a[208:211], v[46:49]\n"
    "ds_write_b64 v213, v[30:31] offset:2048\n"
    "v_mfma_f32_16x16x32_bf16 v[46:49], v[14:17], a[212:215], v[46:49]\n"
    "s_waitcnt lgkmcnt(3)\n"
    "v_fma_f32 v18, v18, s68, v12\n"
    "v_fma_f32 v19, v19, s68, v12\n"
    "v_mfma_f32_16x16x32_bf16 v[4:7], a[0:3], v[136:139], 0\n"
    "s_cmp_eq_u32 s33, 0\n"
    "s_cbranch_scc1 LNOX1%=\n"
    "s_add_u32 m0, s72, 8192\n"
    "s_add_u32 s101, s95, 128\n"
    "buffer_load_dwordx4 v216, s[36:39], s101 offen offset:0 nt lds\n"
    "buffer_load_dwordx4 v217, s[36:39], s101 offen offset:1024 nt lds\n"
    "LNOX1%=:\n"
    "v_fma_f32 v20, v20, s68, v12\n"
    "v_mfma_f32_16x16x32_bf16 v[4:7], a[4:7], v[140:143], v[4:7]\n"
    "v_fma_f32 v21, v21, s68, v12\n"
    "v_fma_f32 v22, v22, s68, v12\n"
    "v_mfma_f32_16x16x32_bf16 v[4:7], a[8:11], v[144:147], v[4:7]\n"
    "v_cvt_pk_bf16_f32 v104, v46, v47\n"
    "v_cvt_pk_bf16_f32 v105, v48, v49\n"
    "v_mfma_f32_16x16x32_bf16 v[4:7], a[12:15], v[148:151], v[4:7]\n"
    "v_exp_f32 v0, v0\n"
    "v_mfma_f32_16x16x32_bf16 v[4:7], a[16:19], v[152:155], v[4:7]\n"
    "v_exp_f32 v1, v1\n"
    "v_mfma_f32_16x16x32_bf16 v[4:7], a[20:23], v[156:159], v[4:7]\n"
    "v_exp_f32 v2, v2\n"
    "v_mfma_f32_16x16x32_bf16 v[8:11], a[24:27], v[136:139], 0\n"
    "v_exp_f32 v3, v3\n"
    "v_mfma_f32_16x16x32_bf16 v[8:11], a[28:31], v[140:143], v[8:11]\n"
    "v_exp_f32 v18, v18\n"
    "v_mfma_f32_16x16x32_bf16 v[8:11], a[32:35], v[144:147], v[8:11]\n"
    "v_exp_f32 v19, v19\n"
    "v_mfma_f32_16x16x32_bf16 v[8:11], a[36:39], v[148:151], v[8:11]\n"
    "v_cvt_pk_bf16_f32 v40, v4, v5\n"
    "v_cvt_pk_bf16_f32 v41, v6, v7\n"
    "v_mfma_f32_16x16x32_bf16 v[8:11], a[40:43], v[152:155], v[8:11]\n"
    "v_exp_f32 v20, v20\n"
    "v_mfma_f32_16x16x32_bf16 v[8:11], a[44:47], v[156:159], v[8:11]\n"
    "v_exp_f32 v21, v21\n"
    "v_mfma_f32_16x16x32_bf16 v[4:7], a[48:51], v[136:139], 0\n"
    "v_fma_f32 v23, v23, s68, v12\n"
    "v_fma_f32 v24, v24, s68, v12\n"
    "v_mfma_f32_16x16x32_bf16 v[4:7], a[52:55], v[140:143], v[4:7]\n"
    "v_fma_f32 v25, v25, s68, v12\n"
    "v_fma_f32 v26, v26, s68, v12\n"
    "v_mfma_f32_16x16x32_bf16 v[4:7], a[56:59], v[144:147], v[4:7]\n"
    "v_cvt_pk_bf16_f32 v42, v8, v9\n"
    "v_cvt_pk_bf16_f32 v43, v10, v11\n"
    "v_mfma_f32_16x16x32_bf16 v[4:7], a[60:63], v[148:151], v[4:7]\n"
    "v_fma_f32 v27, v27, s68, v12\n"
    "v_fma_f32 v28, v28, s68, v12\n"
    "v_mfma_f32_16x16x32_bf16 v[4:7], a[64:67], v[152:155], v[4:7]\n"
    "v_fma_f32 v29, v29, s68, v12\n"
    "v_cvt_pk_bf16_f32 v0, v0, v1\n"
    "v_mfma_f32_16x16x32_bf16 v[4:7], a[68:71], v[156:159], v[4:7]\n"
    "v_cvt_pk_bf16_f32 v1, v2, v3\n"
    "v_cvt_pk_bf16_f32 v2, v18, v19\n"
    "v_mfma_f32_16x16x32_bf16 v[8:11], a[72:75], v[136:139], 0\n"
    "v_cvt_pk_bf16_f32 v3, v20, v21\n"
    "v_fma_f32 v32, v32, s68, v13\n"
    "v_mfma_f32_16x16x32_bf16 v[8:11], a[76:79], v[140:143], v[8:11]\n"
    "v_exp_f32 v22, v22\n"
    "v_mfma_f32_16x16x32_bf16 v[14:17], v[220:223], v[0:3], 0\n"
    "v_cvt_pk_bf16_f32 v56, v4, v5\n"
    "v_cvt_pk_bf16_f32 v57, v6, v7\n"
    "v_mfma_f32_16x16x32_bf16 v[8:11], a[80:83], v[144:147], v[8:11]\n"
    "v_exp_f32 v23, v23\n"
    "v_mfma_f32_16x16x32_bf16 v[8:11], a[84:87], v[148:151], v[8:11]\n"
    "v_exp_f32 v24, v24\n"
    "v_mfma_f32_16x16x32_bf16 v[8:11], a[88:91], v[152:155], v[8:11]\n"
    "v_exp_f32 v25, v25\n"
    "v_mfma_f32_16x16x32_bf16 v[8:11], a[92:95], v[156:159], v[8:11]\n"
    "v_exp_f32 v26, v26\n"
    "v_mfma_f32_16x16x32_bf16 v[4:7], a[96:99], v[136:139], 0\n"
    "v_exp_f32 v27, v27\n"
    "v_mfma_f32_16x16x32_bf16 v[4:7], a[100:103], v[140:143], v[4:7]\n"
    "s_cmp_eq_u32 s33, 0\n"
    "s_cbranch_scc1 LNOX2%=\n"
    "s_add_u32 m0, s72, 16384\n"
    "s_add_u32 s101, s95, 256\n"
    "buffer_load_dwordx4 v216, s[36:39], s101 offen offset:0 nt lds\n"
    "buffer_load_dwordx4 v217, s[36:39], s101 offen offset:1024 nt lds\n"
    "LNOX2%=:\n"
    "v_cvt_pk_bf16_f32 v18, v22, v23\n"
    "v_mfma_f32_16x16x32_bf16 v[4:7], a[104:107], v[144:147], v[4:7]\n"
    "v_exp_f32 v28, v28\n"
    "v_mfma_f32_16x16x32_bf16 v[4:7], a[108:111], v[148:151], v[4:7]\n"
    "v_exp_f32 v29, v29\n"
    "v_mfma_f32_16x16x32_bf16 v[4:7], a[112:115], v[152:155], v[4:7]\n"
    "v_cvt_pk_bf16_f32 v19, v24, v25\n"
    "v_cvt_pk_bf16_f32 v20, v26, v27\n"
    "v_mfma_f32_16x16x32_bf16 v[4:7], a[116:119], v[156:159], v[4:7]\n"
    "v_cvt_pk_bf16_f32 v21, v28, v29\n"
    "v_cvt_pk_bf16_f32 v68, v8, v9\n"
    "v_mfma_f32_16x16x32_bf16 v[22:25], a[120:123], v[136:139], 0\n"
    "v_cvt_pk_bf16_f32 v69, v10, v11\n"
    "v_fma_f32 v33, v33, s68, v13\n"
    "v_mfma_f32_16x16x32_bf16 v[14:17], v[220:223], v[18:21], v[14:17]\n"
    "v_fma_f32 v34, v34, s68, v13\n"
    "v_fma_f32 v35, v35, s68, v13\n"
    "v_mfma_f32_16x16x32_bf16 v[8:11], v[112:115], v[0:3], 0\n"
    "v_cvt_pk_bf16_f32 v70, v4, v5\n"
    "v_cvt_pk_bf16_f32 v71, v6, v7\n"
    "v_mfma_f32_16x16x32_bf16 v[8:11], v[116:119], v[18:21], v[8:11]\n"
    "v_fma_f32 v92, v92, s68, v13\n"
    "v_fma_f32 v93, v93, s68, v13\n"
    "v_mfma_f32_16x16x32_bf16 v[4:7], v[120:123], v[0:3], 0\n"
    "v_rcp_f32 v12, v14\n"
    "v_mfma_f32_16x16x32_bf16 v[4:7], v[124:127], v[18:21], v[4:7]\n"
    "v_fma_f32 v94, v94, s68, v13\n"
    "v_fma_f32 v95, v95, s68, v13\n"
    "v_mfma_f32_16x16x32_bf16 v[14:17], v[128:131], v[0:3], 0\n"
    "v_mul_f32 v8, v8, v12\n"
    "v_mul_f32 v9, v9, v12\n"
    "v_mfma_f32_16x16x32_bf16 v[14:17], v[132:135], v[18:21], v[14:17]\n"
    "v_mul_f32 v10, v10, v12\n"
    "v_mul_f32 v11, v11, v12\n"
    "v_mfma_f32_16x16x32_bf16 v[22:25], a[124:127], v[140:143], v[22:25]\n"
    "v_mul_f32 v4, v4, v12\n"
    "v_mul_f32 v5, v5, v12\n"
    "v_mfma_f32_16x16x32_bf16 v[22:25], a[128:131], v[144:147], v[22:25]\n"
    "v_mul_f32 v6, v6, v12\n"
    "v_mul_f32 v7, v7, v12\n"
    "v_mfma_f32_16x16x32_bf16 v[22:25], a[132:135], v[148:151], v[22:25]\n"
    "ds_read_b128 v[0:3], v205 offset:4096\n"
    "v_mul_f32 v14, v14, v12\n"
    "v_mfma_f32_16x16x32_bf16 v[22:25], a[136:139], v[152:155], v[22:25]\n"
    "ds_read_b128 v[18:21], v206 offset:4096\n"
    "v_mul_f32 v15, v15, v12\n"
    "v_mfma_f32_16x16x32_bf16 v[22:25], a[140:143], v[156:159], v[22:25]\n"
    "ds_read_b128 v[26:29], v205 offset:12288\n"
    "v_mul_f32 v16, v16, v12\n"
    "v_mfma_f32_16x16x32_bf16 v[44:47], v[136:139], a[144:147], 0\n"
    "ds_read_b128 v[48:51], v206 offset:12288\n"
    "v_mul_f32 v17, v17, v12\n"
    "v_mfma_f32_16x16x32_bf16 v[44:47], v[140:143], a[148:151], v[44:47]\n"
    "ds_read_b128 v[76:79], v205 offset:20480\n"
    "v_cvt_pk_bf16_f32 v8, v8, v9\n"
    "v_mfma_f32_16x16x32_bf16 v[44:47], v[144:147], a[152:155], v[44:47]\n"
    "ds_read_b128 v[160:163], v206 offset:20480\n"
    "v_cvt_pk_bf16_f32 v82, v22, v23\n"
    "v_mfma_f32_16x16x32_bf16 v[44:47], v[148:151], a[156:159], v[44:47]\n"
    "v_cvt_pk_bf16_f32 v83, v24, v25\n"
    "v_cvt_pk_bf16_f32 v9, v10, v11\n"
    "v_mfma_f32_16x16x32_bf16 v[44:47], v[152:155], a[160:163], v[44:47]\n"
    "ds_write_b64 v211, v[8:9] offset:4096\n"
    "v_mfma_f32_16x16x32_bf16 v[44:47], v[156:159], a[164:167], v[44:47]\n"
    "v_cvt_pk_bf16_f32 v4, v4, v5\n"
    "v_cvt_pk_bf16_f32 v5, v6, v7\n"
    "v_mfma_f32_16x16x32_bf16 v[6:9], v[136:139], a[168:171], 0\n"
    "v_cvt_pk_bf16_f32 v10, v14, v15\n"
    "v_cvt_pk_bf16_f32 v11, v16, v17\n"
    "v_mfma_f32_16x16x32_bf16 v[6:9], v[140:143], a[172:175], v[6:9]\n"
    "ds_write_b64 v212, v[4:5] offset:4096\n"
    "v_mfma_f32_16x16x32_bf16 v[6:9], v[144:147], a[176:179], v[6:9]\n"
    "v_cvt_pk_bf16_f32 v90, v44, v45\n"
    "v_cvt_pk_bf16_f32 v91, v46, v47\n"
    "v_mfma_f32_16x16x32_bf16 v[6:9], v[148:151], a[180:183], v[6:9]\n"
    "v_exp_f32 v32, v32\n"
    "v_mfma_f32_16x16x32_bf16 v[6:9], v[152:155], a[184:187], v[6:9]\n"
    "ds_write_b64 v213, v[10:11] offset:4096\n"
    "v_mfma_f32_16x16x32_bf16 v[6:9], v[156:159], a[188:191], v[6:9]\n"
    "v_exp_f32 v33, v33\n"
    "v_mfma_f32_16x16x32_bf16 v[14:17], v[136:139], a[192:195], 0\n"
    "v_exp_f32 v34, v34\n"
    "v_mfma_f32_16x16x32_bf16 v[14:17], v[140:143], a[196:199], v[14:17]\n"
    "v_exp_f32 v35, v35\n"
    "v_mfma_f32_16x16x32_bf16 v[14:17], v[144:147], a[200:203], v[14:17]\n"
    "s_cmp_eq_u32 s33, 0\n"
    "s_cbranch_scc1 LNOX3%=\n"
    "s_mov_b32 m0, s73\n"
    "s_nop 0\n"
    "buffer_load_dwordx4 v218, s[40:43], s99 offen offset:0 nt lds\n"
    "buffer_load_dwordx4 v219, s[40:43], s99 offen offset:1024 nt lds\n"
    "LNOX3%=:\n"
    "v_fma_f32 v100, v100, s68, v13\n"
    "v_mfma_f32_16x16x32_bf16 v[14:17], v[148:151], a[204:207], v[14:17]\n"
    "v_cvt_pk_bf16_f32 v98, v6, v7\n"
    "v_cvt_pk_bf16_f32 v99, v8, v9\n"
    "v_mfma_f32_16x16x32_bf16 v[14:17], v[152:155], a[208:211], v[14:17]\n"
    "v_exp_f32 v92, v92\n"
    "v_mfma_f32_16x16x32_bf16 v[14:17], v[156:159], a[212:215], v[14:17]\n"
    "s_waitcnt lgkmcnt(2)\n"
    "v_exp_f32 v93, v93\n"
    "v_mfma_f32_16x16x32_bf16 v[4:7], a[0:3], v[0:3], 0\n"
    "v_exp_f32 v94, v94\n"
    "v_mfma_f32_16x16x32_bf16 v[4:7], a[4:7], v[18:21], v[4:7]\n"
    "v_exp_f32 v95, v95\n"
    "v_mfma_f32_16x16x32_bf16 v[4:7], a[8:11], v[26:29], v[4:7]\n"
    "v_fma_f32 v101, v101, s68, v13\n"
    "v_fma_f32 v102, v102, s68, v13\n"
    "v_mfma_f32_16x16x32_bf16 v[4:7], a[12:15], v[48:51], v[4:7]\n"
    "v_cvt_pk_bf16_f32 v106, v14, v15\n"
    "v_cvt_pk_bf16_f32 v107, v16, v17\n"
    "v_mfma_f32_16x16x32_bf16 v[4:7], a[16:19], v[76:79], v[4:7]\n"
    "v_fma_f32 v103, v103, s68, v13\n"
    "v_fma_f32 v108, v108, s68, v13\n"
    "v_mfma_f32_16x16x32_bf16 v[4:7], a[20:23], v[160:163], v[4:7]\n"
    "v_fma_f32 v109, v109, s68, v13\n"
    "v_fma_f32 v110, v110, s68, v13\n"
    "v_mfma_f32_16x16x32_bf16 v[8:11], a[24:27], v[0:3], 0\n"
    "v_fma_f32 v111, v111, s68, v13\n"
    "v_cvt_pk_bf16_f32 v12, v32, v33\n"
    "v_mfma_f32_16x16x32_bf16 v[8:11], a[28:31], v[18:21], v[8:11]\n"
    "v_cvt_pk_bf16_f32 v13, v34, v35\n"
    "v_cvt_pk_bf16_f32 v14, v92, v93\n"
    "v_mfma_f32_16x16x32_bf16 v[8:11], a[32:35], v[26:29], v[8:11]\n"
    "v_cvt_pk_bf16_f32 v44, v4, v5\n"
    "v_cvt_pk_bf16_f32 v45, v6, v7\n"
    "v_mfma_f32_16x16x32_bf16 v[8:11], a[36:39], v[48:51], v[8:11]\n"
    "v_cvt_pk_bf16_f32 v15, v94, v95\n"
    "v_mov_b64 v[180:181], v[80:81]\n"
    "v_mfma_f32_16x16x32_bf16 v[8:11], a[40:43], v[76:79], v[8:11]\n"
    "v_exp_f32 v100, v100\n"
    "v_mfma_f32_16x16x32_bf16 v[4:7], v[220:223], v[12:15], 0\n"
    "v_exp_f32 v101, v101\n"
    "v_mfma_f32_16x16x32_bf16 v[8:11], a[44:47], v[160:163], v[8:11]\n"
    "v_exp_f32 v102, v102\n"
    "v_mfma_f32_16x16x32_bf16 v[22:25], a[48:51], v[0:3], 0\n"
    "v_exp_f32 v103, v103\n"
    "v_mfma_f32_16x16x32_bf16 v[22:25], a[52:55], v[18:21], v[22:25]\n"
    "v_exp_f32 v108, v108\n"
    "v_mfma_f32_16x16x32_bf16 v[22:25], a[56:59], v[26:29], v[22:25]\n"
    "v_exp_f32 v109, v109\n"
    "v_mfma_f32_16x16x32_bf16 v[22:25], a[60:63], v[48:51], v[22:25]\n"
    "v_exp_f32 v110, v110\n"
    "v_mfma_f32_16x16x32_bf16 v[22:25], a[64:67], v[76:79], v[22:25]\n"
    "v_exp_f32 v111, v111\n"
    "v_mfma_f32_16x16x32_bf16 v[22:25], a[68:71], v[160:163], v[22:25]\n"
    "v_cvt_pk_bf16_f32 v46, v8, v9\n"
    "v_cvt_pk_bf16_f32 v47, v10, v11\n"
    "v_mfma_f32_16x16x32_bf16 v[8:11], a[72:75], v[0:3], 0\n"
    "v_cvt_pk_bf16_f32 v30, v100, v101\n"
    "v_cvt_pk_bf16_f32 v31, v102, v103\n"
    "v_mfma_f32_16x16x32_bf16 v[8:11], a[76:79], v[18:21], v[8:11]\n"
    "v_cvt_pk_bf16_f32 v32, v108, v109\n"
    "v_cvt_pk_bf16_f32 v33, v110, v111\n"
    "v_mfma_f32_16x16x32_bf16 v[8:11], a[80:83], v[26:29], v[8:11]\n"
    "v_cvt_pk_bf16_f32 v58, v22, v23\n"
    "v_cvt_pk_bf16_f32 v59, v24, v25\n"
    "v_mfma_f32_16x16x32_bf16 v[4:7], v[220:223], v[30:33], v[4:7]\n"
    "v_mov_b64 v[182:183], v[82:83]\n"
    "v_mov_b64 v[188:189], v[82:83]\n"
    "v_mfma_f32_16x16x32_bf16 v[22:25], v[112:115], v[12:15], 0\n"
    "v_mov_b64 v[194:195], v[82:83]\n"
    "v_mfma_f32_16x16x32_bf16 v[22:25], v[116:119], v[30:33], v[22:25]\n"
    "v_mfma_f32_16x16x32_bf16 v[84:87], v[120:123], v[12:15], 0\n"
    "v_mfma_f32_16x16x32_bf16 v[84:87], v[124:127], v[30:33], v[84:87]\n"
    "v_mfma_f32_16x16x32_bf16 v[92:95], v[128:131], v[12:15], 0\n"
    "v_rcp_f32 v4, v4\n"
    "v_mfma_f32_16x16x32_bf16 v[92:95], v[132:135], v[30:33], v[92:95]\n"
    "v_mfma_f32_16x16x32_bf16 v[8:11], a[84:87], v[48:51], v[8:11]\n"
    "v_mfma_f32_16x16x32_bf16 v[8:11], a[88:91], v[76:79], v[8:11]\n"
    "v_mfma_f32_16x16x32_bf16 v[8:11], a[92:95], v[160:163], v[8:11]\n"
    "v_mul_f32 v22, v22, v4\n"
    "v_mul_f32 v23, v23, v4\n"
    "v_mfma_f32_16x16x32_bf16 v[12:15], a[96:99], v[0:3], 0\n"
    "v_mul_f32 v24, v24, v4\n"
    "v_mul_f32 v25, v25, v4\n"
    "v_mfma_f32_16x16x32_bf16 v[12:15], a[100:103], v[18:21], v[12:15]\n"
    "v_mul_f32 v84, v84, v4\n"
    "v_mul_f32 v85, v85, v4\n"
    "v_mfma_f32_16x16x32_bf16 v[12:15], a[104:107], v[26:29], v[12:15]\n"
    "v_cvt_pk_bf16_f32 v72, v8, v9\n"
    "v_cvt_pk_bf16_f32 v73, v10, v11\n"
    "v_mfma_f32_16x16x32_bf16 v[12:15], a[108:111], v[48:51], v[12:15]\n"
    "v_mul_f32 v86, v86, v4\n"
    "v_mul_f32 v87, v87, v4\n"
    "v_mfma_f32_16x16x32_bf16 v[12:15], a[112:115], v[76:79], v[12:15]\n"
    "v_mul_f32 v92, v92, v4\n"
    "v_mul_f32 v93, v93, v4\n"
    "v_mfma_f32_16x16x32_bf16 v[12:15], a[116:119], v[160:163], v[12:15]\n"
    "v_mul_f32 v94, v94, v4\n"
    "v_mul_f32 v95, v95, v4\n"
    "v_mfma_f32_16x16x32_bf16 v[4:7], a[120:123], v[0:3], 0\n"
    "v_cvt_pk_bf16_f32 v8, v22, v23\n"
    "v_cvt_pk_bf16_f32 v9, v24, v25\n"
    "v_mfma_f32_16x16x32_bf16 v[4:7], a[124:127], v[18:21], v[4:7]\n"
    "ds_write_b64 v211, v[8:9] offset:6144\n"
    "v_mfma_f32_16x16x32_bf16 v[4:7], a[128:131], v[26:29], v[4:7]\n"
    "v_cvt_pk_bf16_f32 v8, v84, v85\n"
    "v_cvt_pk_bf16_f32 v9, v86, v87\n"
    "v_mfma_f32_16x16x32_bf16 v[4:7], a[132:135], v[48:51], v[4:7]\n"
    "ds_read_b128 v[22:25], v205 offset:6144\n"
    "v_cvt_pk_bf16_f32 v10, v92, v93\n"
    "v_mfma_f32_16x16x32_bf16 v[4:7], a[136:139], v[76:79], v[4:7]\n"
    "ds_write_b64 v212, v[8:9] offset:6144\n"
    "v_mfma_f32_16x16x32_bf16 v[4:7], a[140:143], v[160:163], v[4:7]\n"
    "ds_read_b128 v[30:33], v206 offset:6144\n"
    "v_cvt_pk_bf16_f32 v11, v94, v95\n"
    "v_mfma_f32_16x16x32_bf16 v[92:95], v[0:3], a[144:147], 0\n"
    "ds_read_b128 v[110:113], v205 offset:14336\n"
    "v_cvt_pk_bf16_f32 v74, v12, v13\n"
    "v_mfma_f32_16x16x32_bf16 v[92:95], v[18:21], a[148:151], v[92:95]\n"
    "ds_write_b64 v213, v[10:11] offset:6144\n"
    "v_mfma_f32_16x16x32_bf16 v[92:95], v[26:29], a[152:155], v[92:95]\n"
    "ds_read_b128 v[8:11], v206 offset:14336\n"
    "v_cvt_pk_bf16_f32 v75, v14, v15\n"
    "v_mfma_f32_16x16x32_bf16 v[92:95], v[48:51], a[156:159], v[92:95]\n"
    "ds_read_b128 v[12:15], v205 offset:22528\n"
    "v_cvt_pk_bf16_f32 v84, v4, v5\n"
    "v_mfma_f32_16x16x32_bf16 v[92:95], v[76:79], a[160:163], v[92:95]\n"
    "ds_read_b128 v[114:117], v206 offset:22528\n"
    "v_cvt_pk_bf16_f32 v85, v6, v7\n"
    "v_mfma_f32_16x16x32_bf16 v[92:95], v[160:163], a[164:167], v[92:95]\n"
    "v_mfma_f32_16x16x32_bf16 v[4:7], v[0:3], a[168:171], 0\n"
    "v_mfma_f32_16x16x32_bf16 v[4:7], v[18:21], a[172:175], v[4:7]\n"
    "v_mfma_f32_16x16x32_bf16 v[4:7], v[26:29], a[176:179], v[4:7]\n"
    "v_mfma_f32_16x16x32_bf16 v[4:7], v[48:51], a[180:183], v[4:7]\n"
    "v_mfma_f32_16x16x32_bf16 v[4:7], v[76:79], a[184:187], v[4:7]\n"
    "v_mfma_f32_16x16x32_bf16 v[4:7], v[160:163], a[188:191], v[4:7]\n"
    "v_mfma_f32_16x16x32_bf16 v[118:121], v[0:3], a[192:195], 0\n"
    "v_mfma_f32_16x16x32_bf16 v[118:121], v[18:21], a[196:199], v[118:121]\n"
    "v_cvt_pk_bf16_f32 v92, v92, v93\n"
    "v_cvt_pk_bf16_f32 v93, v94, v95\n"
    "v_mfma_f32_16x16x32_bf16 v[118:121], v[26:29], a[200:203], v[118:121]\n"
    "v_mfma_f32_16x16x32_bf16 v[118:121], v[48:51], a[204:207], v[118:121]\n"
    "v_mfma_f32_16x16x32_bf16 v[118:121], v[76:79], a[208:211], v[118:121]\n"
    "v_mfma_f32_16x16x32_bf16 v[118:121], v[160:163], a[212:215], v[118:121]\n"
    "s_waitcnt lgkmcnt(0)\n"
    "v_cvt_pk_bf16_f32 v100, v4, v5\n"
    "v_cvt_pk_bf16_f32 v101, v6, v7\n"
    "v_mfma_f32_16x16x32_bf16 v[0:3], a[0:3], v[22:25], 0\n"
    "v_mfma_f32_16x16x32_bf16 v[0:3], a[4:7], v[30:33], v[0:3]\n"
    "v_mfma_f32_16x16x32_bf16 v[0:3], a[8:11], v[110:113], v[0:3]\n"
    "v_mfma_f32_16x16x32_bf16 v[0:3], a[12:15], v[8:11], v[0:3]\n"
    "v_mfma_f32_16x16x32_bf16 v[0:3], a[16:19], v[12:15], v[0:3]\n"
    "v_mfma_f32_16x16x32_bf16 v[0:3], a[20:23], v[114:117], v[0:3]\n"
    "v_cvt_pk_bf16_f32 v108, v118, v119\n"
    "v_cvt_pk_bf16_f32 v109, v120, v121\n"
    "v_mfma_f32_16x16x32_bf16 v[4:7], a[24:27], v[22:25], 0\n"
    "v_mfma_f32_16x16x32_bf16 v[4:7], a[28:31], v[30:33], v[4:7]\n"
    "v_mfma_f32_16x16x32_bf16 v[4:7], a[32:35], v[110:113], v[4:7]\n"
    "v_mfma_f32_16x16x32_bf16 v[4:7], a[36:39], v[8:11], v[4:7]\n"
    "v_mfma_f32_16x16x32_bf16 v[4:7], a[40:43], v[12:15], v[4:7]\n"
    "v_mfma_f32_16x16x32_bf16 v[4:7], a[44:47], v[114:117], v[4:7]\n"
    "v_cvt_pk_bf16_f32 v48, v0, v1\n"
    "v_cvt_pk_bf16_f32 v49, v2, v3\n"
    "v_mfma_f32_16x16x32_bf16 v[0:3], a[48:51], v[22:25], 0\n"
    "v_mfma_f32_16x16x32_bf16 v[0:3], a[52:55], v[30:33], v[0:3]\n"
    "v_mfma_f32_16x16x32_bf16 v[0:3], a[56:59], v[110:113], v[0:3]\n"
    "v_mfma_f32_16x16x32_bf16 v[0:3], a[60:63], v[8:11], v[0:3]\n"
    "v_mfma_f32_16x16x32_bf16 v[0:3], a[64:67], v[12:15], v[0:3]\n"
    "v_mfma_f32_16x16x32_bf16 v[0:3], a[68:71], v[114:117], v[0:3]\n"
    "v_cvt_pk_bf16_f32 v50, v4, v5\n"
    "v_cvt_pk_bf16_f32 v51, v6, v7\n"
    "v_mfma_f32_16x16x32_bf16 v[4:7], a[72:75], v[22:25], 0\n"
    "v_mfma_f32_16x16x32_bf16 v[4:7], a[76:79], v[30:33], v[4:7]\n"
    "v_mfma_f32_16x16x32_bf16 v[4:7], a[80:83], v[110:113], v[4:7]\n"
    "v_mfma_f32_16x16x32_bf16 v[4:7], a[84:87], v[8:11], v[4:7]\n"
    "v_mfma_f32_16x16x32_bf16 v[4:7], a[88:91], v[12:15], v[4:7]\n"
    "v_mfma_f32_16x16x32_bf16 v[4:7], a[92:95], v[114:117], v[4:7]\n"
    "v_cvt_pk_bf16_f32 v62, v0, v1\n"
    "v_cvt_pk_bf16_f32 v63, v2, v3\n"
    "v_mfma_f32_16x16x32_bf16 v[0:3], a[96:99], v[22:25], 0\n"
    "v_mfma_f32_16x16x32_bf16 v[0:3], a[100:103], v[30:33], v[0:3]\n"
    "v_mfma_f32_16x16x32_bf16 v[0:3], a[104:107], v[110:113], v[0:3]\n"
    "v_mfma_f32_16x16x32_bf16 v[0:3], a[108:111], v[8:11], v[0:3]\n"
    "v_mfma_f32_16x16x32_bf16 v[0:3], a[112:115], v[12:15], v[0:3]\n"
    "v_mfma_f32_16x16x32_bf16 v[0:3], a[116:119], v[114:117], v[0:3]\n"
    "v_cvt_pk_bf16_f32 v76, v4, v5\n"
    "v_cvt_pk_bf16_f32 v77, v6, v7\n"
    "v_mfma_f32_16x16x32_bf16 v[4:7], a[120:123], v[22:25], 0\n"
    "v_mfma_f32_16x16x32_bf16 v[4:7], a[124:127], v[30:33], v[4:7]\n"
    "v_mfma_f32_16x16x32_bf16 v[4:7], a[128:131], v[110:113], v[4:7]\n"
    "v_mfma_f32_16x16x32_bf16 v[4:7], a[132:135], v[8:11], v[4:7]\n"
    "v_mfma_f32_16x16x32_bf16 v[4:7], a[136:139], v[12:15], v[4:7]\n"
    "v_mfma_f32_16x16x32_bf16 v[4:7], a[140:143], v[114:117], v[4:7]\n"
    "s_waitcnt vmcnt(0)\n"
    "s_barrier\n"
    "v_mfma_f32_16x16x32_bf16 v[16:19], v[22:25], a[144:147], 0\n"
    "v_cvt_pk_bf16_f32 v78, v0, v1\n"
    "v_cvt_pk_bf16_f32 v79, v2, v3\n"
    "v_mfma_f32_16x16x32_bf16 v[16:19], v[30:33], a[148:151], v[16:19]\n"
    "ds_read_b128 v[136:139], v209 offset:0\n"
    "v_mfma_f32_16x16x32_bf16 v[16:19], v[110:113], a[152:155], v[16:19]\n"
    "ds_read_b128 v[140:143], v210 offset:1024\n"
    "v_cvt_pk_bf16_f32 v86, v4, v5\n"
    "v_mfma_f32_16x16x32_bf16 v[16:19], v[8:11], a[156:159], v[16:19]\n"
    "v_cvt_pk_bf16_f32 v87, v6, v7\n"
    "v_mfma_f32_16x16x32_bf16 v[16:19], v[12:15], a[160:163], v[16:19]\n"
    "ds_read_b128 v[144:147], v209 offset:2048\n"
    "v_mfma_f32_16x16x32_bf16 v[16:19], v[114:117], a[164:167], v[16:19]\n"
    "ds_read_b128 v[148:151], v210 offset:3072\n"
    "v_mfma_f32_16x16x32_bf16 v[0:3], v[22:25], a[168:171], 0\n"
    "ds_read_b128 v[170:173], v205 offset:24576\n"
    "v_mfma_f32_16x16x32_bf16 v[0:3], v[30:33], a[172:175], v[0:3]\n"
    "ds_read_b128 v[176:179], v206 offset:24576\n"
    "v_mfma_f32_16x16x32_bf16 v[0:3], v[110:113], a[176:179], v[0:3]\n"
    "ds_read_b128 v[152:155], v209 offset:4096\n"
    "v_mfma_f32_16x16x32_bf16 v[0:3], v[8:11], a[180:183], v[0:3]\n"
    "v_cvt_pk_bf16_f32 v94, v16, v17\n"
    "v_cvt_pk_bf16_f32 v95, v18, v19\n"
    "v_mfma_f32_16x16x32_bf16 v[0:3], v[12:15], a[184:187], v[0:3]\n"
    "ds_read_b128 v[156:159], v210 offset:5120\n"
    "v_mfma_f32_16x16x32_bf16 v[0:3], v[114:117], a[188:191], v[0:3]\n"
    "ds_read_b128 v[160:163], v209 offset:6144\n"
    "v_mfma_f32_16x16x32_bf16 v[196:199], v[22:25], a[192:195], 0\n"
    "ds_read_b128 v[164:167], v210 offset:7168\n"
    "v_mfma_f32_16x16x32_bf16 v[196:199], v[30:33], a[196:199], v[196:199]\n"
    "ds_read_b128 v[184:187], v205 offset:26624\n"
    "v_mfma_f32_16x16x32_bf16 v[196:199], v[110:113], a[200:203], v[196:199]\n"
    "ds_read_b128 v[190:193], v206 offset:26624\n"
    "v_mfma_f32_16x16x32_bf16 v[196:199], v[8:11], a[204:207], v[196:199]\n"
    "v_cvt_pk_bf16_f32 v102, v0, v1\n"
    "v_cvt_pk_bf16_f32 v103, v2, v3\n"
    "v_mfma_f32_16x16x32_bf16 v[196:199], v[12:15], a[208:211], v[196:199]\n"
    "v_mfma_f32_16x16x32_bf16 v[196:199], v[114:117], a[212:215], v[196:199]\n"
    "s_cmp_lg_u32 s33, 0\n"
    "s_cbranch_scc1 LEXTRA%=\n"
    "s_waitcnt lgkmcnt(0)\n"
    "v_mfma_f32_16x16x32_bf16 v[136:139], v[36:39], v[64:67], v[136:139]\n"
    "ds_read_b128 v[0:3], v209 offset:8192\n"
    "v_mov_b64 v[4:5], v[84:85]\n"
    "v_mfma_f32_16x16x32_bf16 v[136:139], v[52:55], v[168:171], v[136:139]\n"
    "ds_read_b128 v[18:21], v210 offset:9216\n"
    "v_mov_b64 v[10:11], v[84:85]\n"
    "v_mfma_f32_16x16x32_bf16 v[140:143], v[40:43], v[64:67], v[140:143]\n"
    "ds_read_b128 v[22:25], v209 offset:10240\n"
    "v_mov_b64 v[16:17], v[84:85]\n"
    "v_mfma_f32_16x16x32_bf16 v[140:143], v[54:57], v[172:175], v[140:143]\n"
    "ds_read_b128 v[26:29], v210 offset:11264\n"
    "v_cvt_pk_bf16_f32 v110, v196, v197\n"
    "v_mfma_f32_16x16x32_bf16 v[144:147], v[44:47], v[64:67], v[144:147]\n"
    "v_max3_f32 v30, v136, v137, v138\n"
    "v_cvt_pk_bf16_f32 v111, v198, v199\n"
    "v_mfma_f32_16x16x32_bf16 v[144:147], v[58:61], v[174:177], v[144:147]\n"
    "ds_read_b128 v[6:9], v205 offset:28672\n"
    "v_mfma_f32_16x16x32_bf16 v[148:151], v[48:51], v[64:67], v[148:151]\n"
    "v_max3_f32 v31, v139, v140, v141\n"
    "v_max3_f32 v30, v30, v142, v143\n"
    "v_mfma_f32_16x16x32_bf16 v[148:151], v[60:63], v[178:181], v[148:151]\n"
    "ds_read_b128 v[12:15], v206 offset:28672\n"
    "v_mov_b64 v[112:113], v[86:87]\n"
    "v_mfma_f32_16x16x32_bf16 v[152:155], v[36:39], v[68:71], v[152:155]\n"
    "v_max3_f32 v31, v31, v144, v145\n"
    "v_max3_f32 v30, v30, v146, v147\n"
    "v_mfma_f32_16x16x32_bf16 v[152:155], v[52:55], v[182:185], v[152:155]\n"
    "ds_read_b128 v[32:35], v209 offset:12288\n"
    "v_mov_b64 v[118:119], v[86:87]\n"
    "v_mfma_f32_16x16x32_bf16 v[156:159], v[40:43], v[68:71], v[156:159]\n"
    "v_max3_f32 v31, v31, v148, v149\n"
    "v_max3_f32 v30, v30, v150, v151\n"
    "v_mfma_f32_16x16x32_bf16 v[156:159], v[54:57], v[186:189], v[156:159]\n"
    "v_max_f32 v30, v30, v31\n"
    "v_mov_b64 v[124:125], v[86:87]\n"
    "v_mfma_f32_16x16x32_bf16 v[160:163], v[44:47], v[68:71], v[160:163]\n"
    "v_max3_f32 v31, v152, v153, v154\n"
    "v_mfma_f32_16x16x32_bf16 v[160:163], v[58:61], v[188:191], v[160:163]\n"
    "ds_read_b128 v[64:67], v210 offset:13312\n"
    "v_mfma_f32_16x16x32_bf16 v[164:167], v[48:51], v[68:71], v[164:167]\n"
    "ds_read_b128 v[68:71], v209 offset:14336\n"
    "v_max3_f32 v80, v155, v156, v157\n"
    "v_mfma_f32_16x16x32_bf16 v[164:167], v[60:63], v[192:195], v[164:167]\n"
    "s_waitcnt lgkmcnt(5)\n"
    "v_max3_f32 v31, v31, v158, v159\n"
    "v_mfma_f32_16x16x32_bf16 v[0:3], v[36:39], v[72:75], v[0:3]\n"
    "ds_read_b128 v[82:85], v210 offset:15360\n"
    "v_max3_f32 v80, v80, v160, v161\n"
    "s_waitcnt lgkmcnt(5)\n"
    "v_mfma_f32_16x16x32_bf16 v[0:3], v[52:55], v[4:7], v[0:3]\n"
    "v_max3_f32 v31, v31, v162, v163\n"
    "ds_read_b128 v[114:117], v205 offset:30720\n"
    "v_mfma_f32_16x16x32_bf16 v[18:21], v[40:43], v[72:75], v[18:21]\n"
    "v_max3_f32 v80, v80, v164, v165\n"
    "v_max3_f32 v31, v31, v166, v167\n"
    "v_mfma_f32_16x16x32_bf16 v[18:21], v[54:57], v[8:11], v[18:21]\n"
    "v_max_f32 v31, v31, v80\n"
    "v_mfma_f32_16x16x32_bf16 v[22:25], v[44:47], v[72:75], v[22:25]\n"
    "ds_read_b128 v[120:123], v206 offset:30720\n"
    "v_max3_f32 v4, v0, v1, v2\n"
    "s_waitcnt lgkmcnt(6)\n"
    "v_mfma_f32_16x16x32_bf16 v[22:25], v[58:61], v[10:13], v[22:25]\n"
    "v_permlane32_swap_b32 v30, v31\n"
    "v_mfma_f32_16x16x32_bf16 v[26:29], v[48:51], v[72:75], v[26:29]\n"
    "v_max_f32 v30, v30, v31\n"
    "v_max3_f32 v5, v3, v18, v19\n"
    "v_mfma_f32_16x16x32_bf16 v[26:29], v[60:63], v[14:17], v[26:29]\n"
    "ds_read_b128 v[6:9], v207 offset:0\n"
    "v_mov_b32 v31, v30\n"
    "s_waitcnt lgkmcnt(6)\n"
    "v_mfma_f32_16x16x32_bf16 v[32:35], v[36:39], v[76:79], v[32:35]\n"
    "ds_read_b128 v[10:13], v208 offset:0\n"
    "v_max3_f32 v4, v4, v20, v21\n"
    "s_waitcnt lgkmcnt(3)\n"
    "v_mfma_f32_16x16x32_bf16 v[32:35], v[52:55], v[112:115], v[32:35]\n"
    "ds_read_b128 v[14:17], v207 offset:8192\n"
    "v_max3_f32 v5, v5, v22, v23\n"
    "v_mfma_f32_16x16x32_bf16 v[64:67], v[40:43], v[76:79], v[64:67]\n"
    "LPG2%=:\n"
    "s_cbranch_execz LWB%=\n"
    "ds_read_b128 v[36:39], v208 offset:8192\n"
    "v_max3_f32 v4, v4, v24, v25\n"
    "v_mfma_f32_16x16x32_bf16 v[64:67], v[54:57], v[116:119], v[64:67]\n"
    "ds_read_b128 v[40:43], v207 offset:16384\n"
    "v_max3_f32 v5, v5, v26, v27\n"
    "v_mfma_f32_16x16x32_bf16 v[68:71], v[44:47], v[76:79], v[68:71]\n"
    "ds_read_b128 v[44:47], v208 offset:16384\n"
    "v_max3_f32 v52, v32, v33, v34\n"
    "s_waitcnt lgkmcnt(6)\n"
    "v_mfma_f32_16x16x32_bf16 v[68:71], v[58:61], v[118:121], v[68:71]\n"
    "v_permlane16_swap_b32 v30, v31\n"
    "v_mfma_f32_16x16x32_bf16 v[82:85], v[48:51], v[76:79], v[82:85]\n"
    "ds_read_b128 v[48:51], v207 offset:2048\n"
    "v_max_f32 v30, v30, v31\n"
    "v_mfma_f32_16x16x32_bf16 v[82:85], v[60:63], v[122:125], v[82:85]\n"
    "ds_read_b128 v[54:57], v208 offset:2048\n"
    "v_mul_f32 v30, s71, v30\n"
    "s_waitcnt lgkmcnt(7)\n"
    "v_mfma_f32_16x16x32_bf16 v[58:61], a[216:219], v[6:9], 0\n"
    "ds_read_b128 v[72:75], v207 offset:10240\n"
    "v_mov_b32 v31, v30\n"
    "s_waitcnt lgkmcnt(7)\n"
    "v_mfma_f32_16x16x32_bf16 v[58:61], a[220:223], v[10:13], v[58:61]\n"
    "ds_read_b128 v[76:79], v208 offset:10240\n"
    "v_max3_f32 v53, v35, v64, v65\n"
    "s_waitcnt lgkmcnt(7)\n"
    "v_mfma_f32_16x16x32_bf16 v[58:61], a[224:227], v[14:17], v[58:61]\n"
    "ds_read_b128 v[112:115], v207 offset:18432\n"
    "v_max3_f32 v52, v52, v66, v67\n"
    "s_waitcnt lgkmcnt(7)\n"
    "v_mfma_f32_16x16x32_bf16 v[58:61], a[228:231], v[36:39], v[58:61]\n"
    "ds_read_b128 v[116:119], v208 offset:18432\n"
    "v_max3_f32 v53, v53, v68, v69\n"
    "s_waitcnt lgkmcnt(7)\n"
    "v_mfma_f32_16x16x32_bf16 v[58:61], a[232:235], v[40:43], v[58:61]\n"
    "s_waitcnt lgkmcnt(6)\n"
    "v_permlane32_swap_b32 v30, v31\n"
    "v_mfma_f32_16x16x32_bf16 v[58:61], a[236:239], v[44:47], v[58:61]\n"
    "v_fma_f32 v136, v136, s68, v30\n"
    "v_fma_f32 v137, v137, s68, v30\n"
    "v_mfma_f32_16x16x32_bf16 v[120:123], a[240:243], v[6:9], 0\n"
    "v_fma_f32 v138, v138, s68, v30\n"
    "v_fma_f32 v139, v139, s68, v30\n"
    "v_mfma_f32_16x16x32_bf16 v[120:123], a[244:247], v[10:13], v[120:123]\n"
    "v_fma_f32 v140, v140, s68, v30\n"
    "v_fma_f32 v141, v141, s68, v30\n"
    "v_mfma_f32_16x16x32_bf16 v[120:123], a[248:251], v[14:17], v[120:123]\n"
    "v_cvt_pk_bf16_f32 v58, v58, v59\n"
    "v_cvt_pk_bf16_f32 v59, v60, v61\n"
    "v_mfma_f32_16x16x32_bf16 v[120:123], a[252:255], v[36:39], v[120:123]\n"
    "v_fma_f32 v142, v142, s68, v30\n"
    "v_fma_f32 v143, v143, s68, v30\n"
    "v_mfma_f32_16x16x32_bf16 v[120:123], v[224:227], v[40:43], v[120:123]\n"
    "v_exp_f32 v136, v136\n"
    "v_mfma_f32_16x16x32_bf16 v[120:123], v[228:231], v[44:47], v[120:123]\n"
    "v_exp_f32 v137, v137\n"
    "v_mfma_f32_16x16x32_bf16 v[124:127], v[232:235], v[6:9], 0\n"
    "v_exp_f32 v138, v138\n"
    "v_mfma_f32_16x16x32_bf16 v[124:127], v[236:239], v[10:13], v[124:127]\n"
    "v_exp_f32 v139, v139\n"
    "v_mfma_f32_16x16x32_bf16 v[124:127], v[240:243], v[14:17], v[124:127]\n"
    "v_exp_f32 v140, v140\n"
    "v_mfma_f32_16x16x32_bf16 v[124:127], v[244:247], v[36:39], v[124:127]\n"
    "v_cvt_pk_bf16_f32 v60, v120, v121\n"
    "v_cvt_pk_bf16_f32 v61, v122, v123\n"
    "v_mfma_f32_16x16x32_bf16 v[124:127], v[248:251], v[40:43], v[124:127]\n"
    "buffer_store_dwordx4 v[58:61], v214, s[44:47], s80 offen offset:0 nt\n"
    "v_mfma_f32_16x16x32_bf16 v[124:127], v[252:255], v[44:47], v[124:127]\n"
    "ds_read_b128 v[6:9], v207 offset:4096\n"
    "v_fma_f32 v144, v144, s68, v30\n"
    "s_waitcnt lgkmcnt(1)\n"
    "v_mfma_f32_16x16x32_bf16 v[10:13], a[216:219], v[48:51], 0\n"
    "ds_read_b128 v[14:17], v208 offset:4096\n"
    "v_fma_f32 v145, v145, s68, v30\n"
    "v_mfma_f32_16x16x32_bf16 v[10:13], a[220:223], v[54:57], v[10:13]\n"
    "ds_read_b128 v[36:39], v207 offset:12288\n"
    "v_fma_f32 v146, v146, s68, v30\n"
    "v_mfma_f32_16x16x32_bf16 v[10:13], a[224:227], v[72:75], v[10:13]\n"
    "ds_read_b128 v[40:43], v208 offset:12288\n"
    "v_fma_f32 v147, v147, s68, v30\n"
    "v_mfma_f32_16x16x32_bf16 v[10:13], a[228:231], v[76:79], v[10:13]\n"
    "ds_read_b128 v[44:47], v207 offset:20480\n"
    "v_fma_f32 v148, v148, s68, v30\n"
    "v_mfma_f32_16x16x32_bf16 v[10:13], a[232:235], v[112:115], v[10:13]\n"
    "ds_read_b128 v[58:61], v208 offset:20480\n"
    "v_fma_f32 v149, v149, s68, v30\n"
    "v_mfma_f32_16x16x32_bf16 v[10:13], a[236:239], v[116:119], v[10:13]\n"
    "v_exp_f32 v141, v141\n"
    "v_mfma_f32_16x16x32_bf16 v[120:123], a[240:243], v[48:51], 0\n"
    "v_exp_f32 v142, v142\n"
    "v_mfma_f32_16x16x32_bf16 v[120:123], a[244:247], v[54:57], v[120:123]\n"
    "v_exp_f32 v143, v143\n"
    "v_mfma_f32_16x16x32_bf16 v[120:123], a[248:251], v[72:75], v[120:123]\n"
    "v_fma_f32 v150, v150, s68, v30\n"
    "v_fma_f32 v151, v151, s68, v30\n"
    "v_mfma_f32_16x16x32_bf16 v[120:123], a[252:255], v[76:79], v[120:123]\n"
    "v_cvt_pk_bf16_f32 v128, v136, v137\n"
    "v_cvt_pk_bf16_f32 v129, v138, v139\n"
    "v_mfma_f32_16x16x32_bf16 v[120:123], v[224:227], v[112:115], v[120:123]\n"
    "v_cvt_pk_bf16_f32 v130, v140, v141\n"
    "v_cvt_pk_bf16_f32 v131, v142, v143\n"
    "v_mfma_f32_16x16x32_bf16 v[120:123], v[228:231], v[116:119], v[120:123]\n"
    "v_exp_f32 v144, v144\n"
    "v_mfma_f32_16x16x32_bf16 v[132:135], v[220:223], v[128:131], 0\n"
    "v_exp_f32 v145, v145\n"
    "v_mfma_f32_16x16x32_bf16 v[136:139], v[232:235], v[48:51], 0\n"
    "v_exp_f32 v146, v146\n"
    "v_mfma_f32_16x16x32_bf16 v[136:139], v[236:239], v[54:57], v[136:139]\n"
    "v_exp_f32 v147, v147\n"
    "v_mfma_f32_16x16x32_bf16 v[136:139], v[240:243], v[72:75], v[136:139]\n"
    "v_exp_f32 v148, v148\n"
    "v_mfma_f32_16x16x32_bf16 v[136:139], v[244:247], v[76:79], v[136:139]\n"
    "v_exp_f32 v149, v149\n"
    "v_mfma_f32_16x16x32_bf16 v[136:139], v[248:251], v[112:115], v[136:139]\n"
    "ds_read_b128 v[48:51], v207 offset:6144\n"
    "v_cvt_pk_bf16_f32 v54, v124, v125\n"
    "v_mfma_f32_16x16x32_bf16 v[136:139], v[252:255], v[116:119], v[136:139]\n"
    "s_barrier\n"
    "s_waitcnt lgkmcnt(1)\n"
    "v_mfma_f32_16x16x32_bf16 v[72:75], a[216:219], v[6:9], 0\n"
    "ds_read_b128 v[76:79], v208 offset:6144\n"
    "v_cvt_pk_bf16_f32 v55, v126, v127\n"
    "v_mfma_f32_16x16x32_bf16 v[72:75], a[220:223], v[14:17], v[72:75]\n"
    "buffer_store_dwordx2 v[54:55], v215, s[44:47], s80 offen offset:0 nt\n"
    "v_mfma_f32_16x16x32_bf16 v[72:75], a[224:227], v[36:39], v[72:75]\n"
    "ds_read_b128 v[112:115], v207 offset:14336\n"
    "v_cvt_pk_bf16_f32 v54, v144, v145\n"
    "v_mfma_f32_16x16x32_bf16 v[72:75], a[228:231], v[40:43], v[72:75]\n"
    "ds_read_b128 v[116:119], v208 offset:14336\n"
    "v_cvt_pk_bf16_f32 v55, v146, v147\n"
    "v_mfma_f32_16x16x32_bf16 v[72:75], a[232:235], v[44:47], v[72:75]\n"
    "ds_read_b128 v[124:127], v207 offset:22528\n"
    "v_cvt_pk_bf16_f32 v56, v148, v149\n"
    "v_mfma_f32_16x16x32_bf16 v[72:75], a[236:239], v[58:61], v[72:75]\n"
    "ds_read_b128 v[140:143], v208 offset:22528\n"
    "v_cvt_pk_bf16_f32 v10, v10, v11\n"
    "v_mfma_f32_16x16x32_bf16 v[144:147], a[240:243], v[6:9], 0\n"
    "v_exp_f32 v150, v150\n"
    "v_mfma_f32_16x16x32_bf16 v[144:147], a[244:247], v[14:17], v[144:147]\n"
    "v_exp_f32 v151, v151\n"
    "v_mfma_f32_16x16x32_bf16 v[144:147], a[248:251], v[36:39], v[144:147]\n"
    "v_cvt_pk_bf16_f32 v57, v150, v151\n"
    "v_cvt_pk_bf16_f32 v11, v12, v13\n"
    "v_mfma_f32_16x16x32_bf16 v[144:147], a[252:255], v[40:43], v[144:147]\n"
    "v_cvt_pk_bf16_f32 v12, v120, v121\n"
    "v_cvt_pk_bf16_f32 v13, v122, v123\n"
    "v_mfma_f32_16x16x32_bf16 v[132:135], v[220:223], v[54:57], v[132:135]\n"
    "buffer_store_dwordx4 v[10:13], v214, s[44:47], s81 offen offset:0 nt\n"
    "v_mfma_f32_16x16x32_bf16 v[120:123], v[88:91], v[128:131], 0\n"
    "v_cvt_pk_bf16_f32 v62, v136, v137\n"
    "v_cvt_pk_bf16_f32 v63, v138, v139\n"
    "v_mfma_f32_16x16x32_bf16 v[120:123], v[92:95], v[54:57], v[120:123]\n"
    "v_max3_f32 v52, v52, v70, v71\n"
    "v_max3_f32 v4, v4, v28, v29\n"
    "v_mfma_f32_16x16x32_bf16 v[10:13], v[96:99], v[128:131], 0\n"
    "buffer_store_dwordx2 v[62:63], v215, s[44:47], s81 offen offset:0 nt\n"
    "v_mfma_f32_16x16x32_bf16 v[10:13], v[100:103], v[54:57], v[10:13]\n"
    "v_rcp_f32 v30, v132\n"
    "v_mfma_f32_16x16x32_bf16 v[132:135], v[104:107], v[128:131], 0\n"
    "v_max3_f32 v53, v53, v82, v83\n"
    "v_mul_f32 v120, v120, v30\n"
    "v_mfma_f32_16x16x32_bf16 v[132:135], v[108:111], v[54:57], v[132:135]\n"
    "v_mul_f32 v121, v121, v30\n"
    "v_mul_f32 v122, v122, v30\n"
    "v_mfma_f32_16x16x32_bf16 v[144:147], v[224:227], v[44:47], v[144:147]\n"
    "v_mul_f32 v123, v123, v30\n"
    "v_mul_f32 v10, v10, v30\n"
    "v_mfma_f32_16x16x32_bf16 v[144:147], v[228:231], v[58:61], v[144:147]\n"
    "v_mul_f32 v11, v11, v30\n"
    "v_mul_f32 v12, v12, v30\n"
    "v_mfma_f32_16x16x32_bf16 v[54:57], v[232:235], v[6:9], 0\n"
    "v_mul_f32 v13, v13, v30\n"
    "v_mul_f32 v132, v132, v30\n"
    "v_mfma_f32_16x16x32_bf16 v[54:57], v[236:239], v[14:17], v[54:57]\n"
    "v_mul_f32 v133, v133, v30\n"
    "v_mul_f32 v134, v134, v30\n"
    "v_mfma_f32_16x16x32_bf16 v[54:57], v[240:243], v[36:39], v[54:57]\n"
    "v_mul_f32 v135, v135, v30\n"
    "v_cvt_pk_bf16_f32 v6, v120, v121\n"
    "v_mfma_f32_16x16x32_bf16 v[54:57], v[244:247], v[40:43], v[54:57]\n"
    "v_cvt_pk_bf16_f32 v7, v122, v123\n"
    "v_cvt_pk_bf16_f32 v8, v10, v11\n"
    "v_mfma_f32_16x16x32_bf16 v[54:57], v[248:251], v[44:47], v[54:57]\n"
    "ds_write_b64 v211, v[6:7] offset:0\n"
    "v_mfma_f32_16x16x32_bf16 v[54:57], v[252:255], v[58:61], v[54:57]\n"
    "s_waitcnt lgkmcnt(1)\n"
    "v_cvt_pk_bf16_f32 v9, v12, v13\n"
    "v_cvt_pk_bf16_f32 v6, v132, v133\n"
    "v_mfma_f32_16x16x32_bf16 v[10:13], a[216:219], v[48:51], 0\n"
    "v_cvt_pk_bf16_f32 v7, v134, v135\n"
    "v_max3_f32 v52, v52, v84, v85\n"
    "v_mfma_f32_16x16x32_bf16 v[10:13], a[220:223], v[76:79], v[10:13]\n"
    "ds_write_b64 v212, v[8:9] offset:0\n"
    "v_mfma_f32_16x16x32_bf16 v[10:13], a[224:227], v[112:115], v[10:13]\n"
    "v_max_f32 v4, v4, v5\n"
    "v_max_f32 v52, v52, v53\n"
    "v_mfma_f32_16x16x32_bf16 v[10:13], a[228:231], v[116:119], v[10:13]\n"
    "v_cvt_pk_bf16_f32 v14, v72, v73\n"
    "v_cvt_pk_bf16_f32 v15, v74, v75\n"
    "v_mfma_f32_16x16x32_bf16 v[10:13], a[232:235], v[124:127], v[10:13]\n"
    "ds_write_b64 v213, v[6:7] offset:0\n"
    "v_mfma_f32_16x16x32_bf16 v[10:13], a[236:239], v[140:143], v[10:13]\n"
    "v_permlane32_swap_b32 v4, v52\n"
    "v_mfma_f32_16x16x32_bf16 v[6:9], a[240:243], v[48:51], 0\n"
    "v_max_f32 v4, v4, v52\n"
    "v_fma_f32 v152, v152, s68, v31\n"
    "v_mfma_f32_16x16x32_bf16 v[6:9], a[244:247], v[76:79], v[6:9]\n"
    "v_fma_f32 v153, v153, s68, v31\n"
    "v_fma_f32 v154, v154, s68, v31\n"
    "v_mfma_f32_16x16x32_bf16 v[6:9], a[248:251], v[112:115], v[6:9]\n"
    "v_fma_f32 v155, v155, s68, v31\n"
    "v_fma_f32 v156, v156, s68, v31\n"
    "v_mfma_f32_16x16x32_bf16 v[6:9], a[252:255], v[116:119], v[6:9]\n"
    "v_fma_f32 v157, v157, s68, v31\n"
    "v_fma_f32 v158, v158, s68, v31\n"
    "v_mfma_f32_16x16x32_bf16 v[6:9], v[224:227], v[124:127], v[6:9]\n"
    "v_fma_f32 v159, v159, s68, v31\n"
    "v_fma_f32 v160, v160, s68, v31\n"
    "v_mfma_f32_16x16x32_bf16 v[6:9], v[228:231], v[140:143], v[6:9]\n"
    "v_exp_f32 v152, v152\n"
    "v_mfma_f32_16x16x32_bf16 v[36:39], v[232:235], v[48:51], 0\n"
    "v_exp_f32 v153, v153\n"
    "v_mfma_f32_16x16x32_bf16 v[36:39], v[236:239], v[76:79], v[36:39]\n"
    "v_exp_f32 v154, v154\n"
    "v_mfma_f32_16x16x32_bf16 v[36:39], v[240:243], v[112:115], v[36:39]\n"
    "v_exp_f32 v155, v155\n"
    "v_mfma_f32_16x16x32_bf16 v[36:39], v[244:247], v[116:119], v[36:39]\n"
    "v_exp_f32 v156, v156\n"
    "v_mfma_f32_16x16x32_bf16 v[36:39], v[248:251], v[124:127], v[36:39]\n"
    "v_exp_f32 v157, v157\n"
    "v_mfma_f32_16x16x32_bf16 v[36:39], v[252:255], v[140:143], v[36:39]\n"
    "v_exp_f32 v158, v158\n"
    "v_exp_f32 v159, v159\n"
    "v_fma_f32 v161, v161, s68, v31\n"
    "v_fma_f32 v162, v162, s68, v31\n"
    "v_fma_f32 v163, v163, s68, v31\n"
    "v_fma_f32 v164, v164, s68, v31\n"
    "v_fma_f32 v165, v165, s68, v31\n"
    "v_fma_f32 v166, v166, s68, v31\n"
    "v_fma_f32 v167, v167, s68, v31\n"
    "v_cvt_pk_bf16_f32 v40, v152, v153\n"
    "v_cvt_pk_bf16_f32 v41, v154, v155\n"
    "v_cvt_pk_bf16_f32 v42, v156, v157\n"
    "v_cvt_pk_bf16_f32 v43, v158, v159\n"
    "v_exp_f32 v160, v160\n"
    "v_exp_f32 v161, v161\n"
    "v_mfma_f32_16x16x32_bf16 v[44:47], v[220:223], v[40:43], 0\n"
    "v_exp_f32 v162, v162\n"
    "v_exp_f32 v163, v163\n"
    "v_exp_f32 v164, v164\n"
    "v_exp_f32 v165, v165\n"
    "v_exp_f32 v166, v166\n"
    "v_exp_f32 v167, v167\n"
    "v_cvt_pk_bf16_f32 v16, v144, v145\n"
    "v_cvt_pk_bf16_f32 v17, v146, v147\n"
    "v_cvt_pk_bf16_f32 v48, v160, v161\n"
    "v_cvt_pk_bf16_f32 v49, v162, v163\n"
    "v_cvt_pk_bf16_f32 v50, v164, v165\n"
    "v_cvt_pk_bf16_f32 v51, v166, v167\n"
    "v_mov_b32 v52, v4\n"
    "v_cvt_pk_bf16_f32 v30, v54, v55\n"
    "v_mfma_f32_16x16x32_bf16 v[44:47], v[220:223], v[48:51], v[44:47]\n"
    "buffer_store_dwordx4 v[14:17], v214, s[44:47], s82 offen offset:0 nt\n"
    "v_mfma_f32_16x16x32_bf16 v[58:61], v[88:91], v[40:43], 0\n"
    "v_cvt_pk_bf16_f32 v31, v56, v57\n"
    "v_cvt_pk_bf16_f32 v10, v10, v11\n"
    "v_mfma_f32_16x16x32_bf16 v[58:61], v[92:95], v[48:51], v[58:61]\n"
    "v_cvt_pk_bf16_f32 v11, v12, v13\n"
    "v_cvt_pk_bf16_f32 v12, v6, v7\n"
    "v_mfma_f32_16x16x32_bf16 v[14:17], v[96:99], v[40:43], 0\n"
    "buffer_store_dwordx2 v[30:31], v215, s[44:47], s82 offen offset:0 nt\n"
    "v_mfma_f32_16x16x32_bf16 v[14:17], v[100:103], v[48:51], v[14:17]\n"
    "v_rcp_f32 v5, v44\n"
    "v_mfma_f32_16x16x32_bf16 v[44:47], v[104:107], v[40:43], 0\n"
    "v_permlane16_swap_b32 v4, v52\n"
    "v_mfma_f32_16x16x32_bf16 v[44:47], v[108:111], v[48:51], v[44:47]\n"
    "v_mul_f32 v58, v58, v5\n"
    "v_mul_f32 v59, v59, v5\n"
    "v_mul_f32 v60, v60, v5\n"
    "v_mul_f32 v61, v61, v5\n"
    "v_mul_f32 v14, v14, v5\n"
    "v_mul_f32 v15, v15, v5\n"
    "v_mul_f32 v16, v16, v5\n"
    "v_mul_f32 v17, v17, v5\n"
    "v_mul_f32 v44, v44, v5\n"
    "v_mul_f32 v45, v45, v5\n"
    "v_mul_f32 v46, v46, v5\n"
    "v_mul_f32 v47, v47, v5\n"
    "v_cvt_pk_bf16_f32 v6, v58, v59\n"
    "v_cvt_pk_bf16_f32 v7, v60, v61\n"
    "v_cvt_pk_bf16_f32 v14, v14, v15\n"
    "v_cvt_pk_bf16_f32 v15, v16, v17\n"
    "v_cvt_pk_bf16_f32 v16, v44, v45\n"
    "v_cvt_pk_bf16_f32 v17, v46, v47\n"
    "ds_write_b64 v211, v[6:7] offset:2048\n"
    "ds_write_b64 v212, v[14:15] offset:2048\n"
    "ds_write_b64 v213, v[16:17] offset:2048\n"
    "v_cvt_pk_bf16_f32 v13, v8, v9\n"
    "v_cvt_pk_bf16_f32 v6, v36, v37\n"
    "v_cvt_pk_bf16_f32 v7, v38, v39\n"
    "v_max_f32 v4, v4, v52\n"
    "buffer_store_dwordx4 v[10:13], v214, s[44:47], s83 offen offset:0 nt\n"
    "v_mul_f32 v4, s71, v4\n"
    "s_waitcnt lgkmcnt(0)\n"
    "s_barrier\n"
    "ds_read_b128 v[8:11], v207 offset:0\n"
    "ds_read_b128 v[12:15], v208 offset:0\n"
    "ds_read_b128 v[36:39], v207 offset:8192\n"
    "ds_read_b128 v[40:43], v208 offset:8192\n"
    "ds_read_b128 v[44:47], v207 offset:16384\n"
    "ds_read_b128 v[48:51], v208 offset:16384\n"
    "ds_read_b128 v[54:57], v207 offset:2048\n"
    "ds_read_b128 v[58:61], v208 offset:2048\n"
    "ds_read_b128 v[72:75], v207 offset:10240\n"
    "v_mov_b32 v52, v4\n"
    "buffer_store_dwordx2 v[6:7], v215, s[44:47], s83 offen offset:0 nt\n"
    "s_waitcnt lgkmcnt(8)\n"
    "v_mfma_f32_16x16x32_bf16 v[76:79], a[216:219], v[8:11], 0\n"
    "ds_read_b128 v[112:115], v208 offset:10240\n"
    "s_waitcnt lgkmcnt(8)\n"
    "v_mfma_f32_16x16x32_bf16 v[76:79], a[220:223], v[12:15], v[76:79]\n"
    "ds_read_b128 v[116:119], v207 offset:18432\n"
    "s_waitcnt lgkmcnt(8)\n"
    "v_mfma_f32_16x16x32_bf16 v[76:79], a[224:227], v[36:39], v[76:79]\n"
    "ds_read_b128 v[120:123], v208 offset:18432\n"
    "s_waitcnt lgkmcnt(8)\n"
    "v_mfma_f32_16x16x32_bf16 v[76:79], a[228:231], v[40:43], v[76:79]\n"
    "s_waitcnt lgkmcnt(7)\n"
    "v_permlane32_swap_b32 v4, v52\n"
    "v_mfma_f32_16x16x32_bf16 v[76:79], a[232:235], v[44:47], v[76:79]\n"
    "s_waitcnt lgkmcnt(6)\n"
    "v_fma_f32 v0, v0, s68, v4\n"
    "v_fma_f32 v1, v1, s68, v4\n"
    "v_mfma_f32_16x16x32_bf16 v[76:79], a[236:239], v[48:51], v[76:79]\n"
    "v_fma_f32 v2, v2, s68, v4\n"
    "v_fma_f32 v3, v3, s68, v4\n"
    "v_mfma_f32_16x16x32_bf16 v[124:127], a[240:243], v[8:11], 0\n"
    "v_fma_f32 v18, v18, s68, v4\n"
    "v_fma_f32 v19, v19, s68, v4\n"
    "v_mfma_f32_16x16x32_bf16 v[124:127], a[244:247], v[12:15], v[124:127]\n"
    "v_fma_f32 v20, v20, s68, v4\n"
    "v_fma_f32 v21, v21, s68, v4\n"
    "v_mfma_f32_16x16x32_bf16 v[124:127], a[248:251], v[36:39], v[124:127]\n"
    "v_exp_f32 v0, v0\n"
    "v_mfma_f32_16x16x32_bf16 v[124:127], a[252:255], v[40:43], v[124:127]\n"
    "v_exp_f32 v1, v1\n"
    "v_mfma_f32_16x16x32_bf16 v[124:127], v[224:227], v[44:47], v[124:127]\n"
    "v_exp_f32 v2, v2\n"
    "v_mfma_f32_16x16x32_bf16 v[124:127], v[228:231], v[48:51], v[124:127]\n"
    "v_exp_f32 v3, v3\n"
    "v_mfma_f32_16x16x32_bf16 v[128:131], v[232:235], v[8:11], 0\n"
    "v_exp_f32 v18, v18\n"
    "v_mfma_f32_16x16x32_bf16 v[128:131], v[236:239], v[12:15], v[128:131]\n"
    "v_exp_f32 v19, v19\n"
    "v_mfma_f32_16x16x32_bf16 v[128:131], v[240:243], v[36:39], v[128:131]\n"
    "v_exp_f32 v20, v20\n"
    "v_mfma_f32_16x16x32_bf16 v[128:131], v[244:247], v[40:43], v[128:131]\n"
    "v_exp_f32 v21, v21\n"
    "v_mfma_f32_16x16x32_bf16 v[128:131], v[248:251], v[44:47], v[128:131]\n"
    "v_fma_f32 v22, v22, s68, v4\n"
    "v_fma_f32 v23, v23, s68, v4\n"
    "v_mfma_f32_16x16x32_bf16 v[128:131], v[252:255], v[48:51], v[128:131]\n"
    "s_waitcnt lgkmcnt(0)\n"
    "v_fma_f32 v24, v24, s68, v4\n"
    "v_fma_f32 v25, v25, s68, v4\n"
    "v_mfma_f32_16x16x32_bf16 v[6:9], a[216:219], v[54:57], 0\n"
    "v_fma_f32 v26, v26, s68, v4\n"
    "v_fma_f32 v27, v27, s68, v4\n"
    "v_mfma_f32_16x16x32_bf16 v[6:9], a[220:223], v[58:61], v[6:9]\n"
    "v_fma_f32 v28, v28, s68, v4\n"
    "v_fma_f32 v29, v29, s68, v4\n"
    "v_mfma_f32_16x16x32_bf16 v[6:9], a[224:227], v[72:75], v[6:9]\n"
    "v_cvt_pk_bf16_f32 v0, v0, v1\n"
    "v_cvt_pk_bf16_f32 v1, v2, v3\n"
    "v_mfma_f32_16x16x32_bf16 v[6:9], a[228:231], v[112:115], v[6:9]\n"
    "v_cvt_pk_bf16_f32 v2, v18, v19\n"
    "v_cvt_pk_bf16_f32 v3, v20, v21\n"
    "v_mfma_f32_16x16x32_bf16 v[6:9], a[232:235], v[116:119], v[6:9]\n"
    "v_exp_f32 v22, v22\n"
    "v_mfma_f32_16x16x32_bf16 v[10:13], v[220:223], v[0:3], 0\n"
    "v_exp_f32 v23, v23\n"
    "v_mfma_f32_16x16x32_bf16 v[6:9], a[236:239], v[120:123], v[6:9]\n"
    "v_exp_f32 v24, v24\n"
    "v_mfma_f32_16x16x32_bf16 v[14:17], a[240:243], v[54:57], 0\n"
    "v_exp_f32 v25, v25\n"
    "v_mfma_f32_16x16x32_bf16 v[14:17], a[244:247], v[58:61], v[14:17]\n"
    "v_exp_f32 v26, v26\n"
    "v_mfma_f32_16x16x32_bf16 v[14:17], a[248:251], v[72:75], v[14:17]\n"
    "v_exp_f32 v27, v27\n"
    "v_mfma_f32_16x16x32_bf16 v[14:17], a[252:255], v[112:115], v[14:17]\n"
    "v_exp_f32 v28, v28\n"
    "v_mfma_f32_16x16x32_bf16 v[14:17], v[224:227], v[116:119], v[14:17]\n"
    "v_exp_f32 v29, v29\n"
    "v_mfma_f32_16x16x32_bf16 v[14:17], v[228:231], v[120:123], v[14:17]\n"
    "v_cvt_pk_bf16_f32 v18, v22, v23\n"
    "v_cvt_pk_bf16_f32 v19, v24, v25\n"
    "v_mfma_f32_16x16x32_bf16 v[22:25], v[232:235], v[54:57], 0\n"
    "v_cvt_pk_bf16_f32 v20, v26, v27\n"
    "v_cvt_pk_bf16_f32 v21, v28, v29\n"
    "v_mfma_f32_16x16x32_bf16 v[22:25], v[236:239], v[58:61], v[22:25]\n"
    "v_cvt_pk_bf16_f32 v26, v76, v77\n"
    "v_cvt_pk_bf16_f32 v27, v78, v79\n"
    "v_mfma_f32_16x16x32_bf16 v[10:13], v[220:223], v[18:21], v[10:13]\n"
    "v_cvt_pk_bf16_f32 v28, v124, v125\n"
    "v_cvt_pk_bf16_f32 v29, v126, v127\n"
    "v_mfma_f32_16x16x32_bf16 v[36:39], v[88:91], v[0:3], 0\n"
    "buffer_store_dwordx4 v[26:29], v214, s[44:47], s84 offen offset:0 nt\n"
    "v_mfma_f32_16x16x32_bf16 v[36:39], v[92:95], v[18:21], v[36:39]\n"
    "v_cvt_pk_bf16_f32 v4, v128, v129\n"
    "v_cvt_pk_bf16_f32 v5, v130, v131\n"
    "v_mfma_f32_16x16x32_bf16 v[26:29], v[96:99], v[0:3], 0\n"
    "v_rcp_f32 v10, v10\n"
    "v_mfma_f32_16x16x32_bf16 v[26:29], v[100:103], v[18:21], v[26:29]\n"
    "buffer_store_dwordx2 v[4:5], v215, s[44:47], s84 offen offset:0 nt\n"
    "v_mfma_f32_16x16x32_bf16 v[40:43], v[104:107], v[0:3], 0\n"
    "v_cvt_pk_bf16_f32 v0, v6, v7\n"
    "v_mul_f32 v36, v36, v10\n"
    "v_mfma_f32_16x16x32_bf16 v[40:43], v[108:111], v[18:21], v[40:43]\n"
    "v_mul_f32 v37, v37, v10\n"
    "v_mul_f32 v38, v38, v10\n"
    "v_mfma_f32_16x16x32_bf16 v[22:25], v[240:243], v[72:75], v[22:25]\n"
    "v_mul_f32 v39, v39, v10\n"
    "v_mul_f32 v26, v26, v10\n"
    "v_mfma_f32_16x16x32_bf16 v[22:25], v[244:247], v[112:115], v[22:25]\n"
    "v_mul_f32 v27, v27, v10\n"
    "v_mul_f32 v28, v28, v10\n"
    "v_mfma_f32_16x16x32_bf16 v[22:25], v[248:251], v[116:119], v[22:25]\n"
    "v_mul_f32 v29, v29, v10\n"
    "v_mul_f32 v40, v40, v10\n"
    "v_mul_f32 v41, v41, v10\n"
    "v_mul_f32 v42, v42, v10\n"
    "v_mul_f32 v43, v43, v10\n"
    "v_cvt_pk_bf16_f32 v2, v36, v37\n"
    "v_cvt_pk_bf16_f32 v3, v38, v39\n"
    "v_cvt_pk_bf16_f32 v4, v26, v27\n"
    "v_cvt_pk_bf16_f32 v5, v28, v29\n"
    "v_cvt_pk_bf16_f32 v6, v40, v41\n"
    "v_cvt_pk_bf16_f32 v7, v42, v43\n"
    "ds_write_b64 v211, v[2:3] offset:4096\n"
    "ds_write_b64 v212, v[4:5] offset:4096\n"
    "ds_write_b64 v213, v[6:7] offset:4096\n"
    "v_mfma_f32_16x16x32_bf16 v[22:25], v[252:255], v[120:123], v[22:25]\n"
    "v_cvt_pk_bf16_f32 v1, v8, v9\n"
    "v_cvt_pk_bf16_f32 v2, v14, v15\n"
    "v_cvt_pk_bf16_f32 v3, v16, v17\n"
    "v_fma_f32 v32, v32, s68, v52\n"
    "v_fma_f32 v33, v33, s68, v52\n"
    "v_fma_f32 v34, v34, s68, v52\n"
    "v_fma_f32 v35, v35, s68, v52\n"
    "v_fma_f32 v64, v64, s68, v52\n"
    "s_waitcnt lgkmcnt(0)\n"
    "s_barrier\n"
    "ds_read_b128 v[4:7], v207 offset:4096\n"
    "ds_read_b128 v[8:11], v208 offset:4096\n"
    "ds_read_b128 v[12:15], v207 offset:12288\n"
    "ds_read_b128 v[16:19], v208 offset:12288\n"
    "ds_read_b128 v[26:29], v207 offset:20480\n"
    "ds_read_b128 v[36:39], v208 offset:20480\n"
    "v_cvt_pk_bf16_f32 v20, v22, v23\n"
    "v_cvt_pk_bf16_f32 v21, v24, v25\n"
    "v_fma_f32 v65, v65, s68, v52\n"
    "v_fma_f32 v66, v66, s68, v52\n"
    "v_fma_f32 v67, v67, s68, v52\n"
    "v_exp_f32 v32, v32\n"
    "v_exp_f32 v33, v33\n"
    "v_exp_f32 v34, v34\n"
    "v_exp_f32 v35, v35\n"
    "v_exp_f32 v64, v64\n"
    "v_fma_f32 v68, v68, s68, v52\n"
    "s_waitcnt lgkmcnt(5)\n"
    "v_mfma_f32_16x16x32_bf16 v[22:25], a[216:219], v[4:7], 0\n"
    "buffer_store_dwordx4 v[0:3], v214, s[44:47], s85 offen offset:0 nt\n"
    "s_waitcnt lgkmcnt(4)\n"
    "v_mfma_f32_16x16x32_bf16 v[22:25], a[220:223], v[8:11], v[22:25]\n"
    "s_waitcnt lgkmcnt(3)\n"
    "v_exp_f32 v65, v65\n"
    "v_mfma_f32_16x16x32_bf16 v[22:25], a[224:227], v[12:15], v[22:25]\n"
    "s_waitcnt lgkmcnt(2)\n"
    "v_exp_f32 v66, v66\n"
    "v_mfma_f32_16x16x32_bf16 v[22:25], a[228:231], v[16:19], v[22:25]\n"
    "buffer_store_dwordx2 v[20:21], v215, s[44:47], s85 offen offset:0 nt\n"
    "s_waitcnt lgkmcnt(1)\n"
    "v_mfma_f32_16x16x32_bf16 v[22:25], a[232:235], v[26:29], v[22:25]\n"
    "s_waitcnt lgkmcnt(0)\n"
    "v_exp_f32 v67, v67\n"
    "v_mfma_f32_16x16x32_bf16 v[22:25], a[236:239], v[36:39], v[22:25]\n"
    "v_fma_f32 v69, v69, s68, v52\n"
    "v_fma_f32 v70, v70, s68, v52\n"
    "v_mfma_f32_16x16x32_bf16 v[0:3], a[240:243], v[4:7], 0\n"
    "v_fma_f32 v71, v71, s68, v52\n"
    "v_fma_f32 v82, v82, s68, v52\n"
    "v_mfma_f32_16x16x32_bf16 v[0:3], a[244:247], v[8:11], v[0:3]\n"
    "v_fma_f32 v83, v83, s68, v52\n"
    "v_fma_f32 v84, v84, s68, v52\n"
    "v_mfma_f32_16x16x32_bf16 v[0:3], a[248:251], v[12:15], v[0:3]\n"
    "v_fma_f32 v85, v85, s68, v52\n"
    "v_cvt_pk_bf16_f32 v30, v32, v33\n"
    "v_mfma_f32_16x16x32_bf16 v[0:3], a[252:255], v[16:19], v[0:3]\n"
    "v_cvt_pk_bf16_f32 v31, v34, v35\n"
    "v_cvt_pk_bf16_f32 v32, v64, v65\n"
    "v_mfma_f32_16x16x32_bf16 v[0:3], v[224:227], v[26:29], v[0:3]\n"
    "v_cvt_pk_bf16_f32 v33, v66, v67\n"
    "v_cvt_pk_bf16_f32 v20, v22, v23\n"
    "v_mfma_f32_16x16x32_bf16 v[0:3], v[228:231], v[36:39], v[0:3]\n"
    "v_exp_f32 v68, v68\n"
    "v_mfma_f32_16x16x32_bf16 v[40:43], v[220:223], v[30:33], 0\n"
    "v_exp_f32 v69, v69\n"
    "v_mfma_f32_16x16x32_bf16 v[44:47], v[232:235], v[4:7], 0\n"
    "v_exp_f32 v70, v70\n"
    "v_mfma_f32_16x16x32_bf16 v[44:47], v[236:239], v[8:11], v[44:47]\n"
    "v_exp_f32 v71, v71\n"
    "v_mfma_f32_16x16x32_bf16 v[44:47], v[240:243], v[12:15], v[44:47]\n"
    "v_exp_f32 v82, v82\n"
    "v_mfma_f32_16x16x32_bf16 v[44:47], v[244:247], v[16:19], v[44:47]\n"
    "v_exp_f32 v83, v83\n"
    "v_mfma_f32_16x16x32_bf16 v[44:47], v[248:251], v[26:29], v[44:47]\n"
    "v_exp_f32 v84, v84\n"
    "v_mfma_f32_16x16x32_bf16 v[44:47], v[252:255], v[36:39], v[44:47]\n"
    "v_exp_f32 v85, v85\n"
    "v_cvt_pk_bf16_f32 v4, v68, v69\n"
    "v_cvt_pk_bf16_f32 v5, v70, v71\n"
    "v_cvt_pk_bf16_f32 v6, v82, v83\n"
    "v_cvt_pk_bf16_f32 v7, v84, v85\n"
    "v_cvt_pk_bf16_f32 v21, v24, v25\n"
    "v_cvt_pk_bf16_f32 v22, v0, v1\n"
    "v_mfma_f32_16x16x32_bf16 v[40:43], v[220:223], v[4:7], v[40:43]\n"
    "v_cvt_pk_bf16_f32 v23, v2, v3\n"
    "v_cvt_pk_bf16_f32 v0, v44, v45\n"
    "v_mfma_f32_16x16x32_bf16 v[8:11], v[88:91], v[30:33], 0\n"
    "buffer_store_dwordx4 v[20:23], v214, s[44:47], s86 offen offset:0 nt\n"
    "v_mfma_f32_16x16x32_bf16 v[8:11], v[92:95], v[4:7], v[8:11]\n"
    "v_cvt_pk_bf16_f32 v1, v46, v47\n"
    "v_mfma_f32_16x16x32_bf16 v[12:15], v[96:99], v[30:33], 0\n"
    "v_mfma_f32_16x16x32_bf16 v[12:15], v[100:103], v[4:7], v[12:15]\n"
    "v_rcp_f32 v2, v40\n"
    "v_mfma_f32_16x16x32_bf16 v[16:19], v[104:107], v[30:33], 0\n"
    "buffer_store_dwordx2 v[0:1], v215, s[44:47], s86 offen offset:0 nt\n"
    "v_mfma_f32_16x16x32_bf16 v[16:19], v[108:111], v[4:7], v[16:19]\n"
    "s_nop 0\n"
    "v_mul_f32 v8, v8, v2\n"
    "v_mul_f32 v9, v9, v2\n"
    "v_mul_f32 v10, v10, v2\n"
    "v_mul_f32 v11, v11, v2\n"
    "v_mul_f32 v12, v12, v2\n"
    "v_mul_f32 v13, v13, v2\n"
    "v_mul_f32 v14, v14, v2\n"
    "v_mul_f32 v15, v15, v2\n"
    "v_mul_f32 v16, v16, v2\n"
    "v_mul_f32 v17, v17, v2\n"
    "v_mul_f32 v18, v18, v2\n"
    "v_mul_f32 v19, v19, v2\n"
    "v_cvt_pk_bf16_f32 v0, v8, v9\n"
    "v_cvt_pk_bf16_f32 v1, v10, v11\n"
    "v_cvt_pk_bf16_f32 v2, v12, v13\n"
    "v_cvt_pk_bf16_f32 v3, v14, v15\n"
    "v_cvt_pk_bf16_f32 v4, v16, v17\n"
    "v_cvt_pk_bf16_f32 v5, v18, v19\n"
    "ds_write_b64 v211, v[0:1] offset:6144\n"
    "ds_write_b64 v212, v[2:3] offset:6144\n"
    "ds_write_b64 v213, v[4:5] offset:6144\n"
    "s_waitcnt lgkmcnt(0)\n"
    "s_barrier\n"
    "ds_read_b128 v[0:3], v207 offset:6144\n"
    "ds_read_b128 v[4:7], v208 offset:6144\n"
    "ds_read_b128 v[8:11], v207 offset:14336\n"
    "ds_read_b128 v[12:15], v208 offset:14336\n"
    "ds_read_b128 v[16:19], v207 offset:22528\n"
    "ds_read_b128 v[20:23], v208 offset:22528\n"
    "s_waitcnt lgkmcnt(5)\n"
    "v_mfma_f32_16x16x32_bf16 v[24:27], a[216:219], v[0:3], 0\n"
    "s_waitcnt lgkmcnt(4)\n"
    "v_mfma_f32_16x16x32_bf16 v[24:27], a[220:223], v[4:7], v[24:27]\n"
    "s_waitcnt lgkmcnt(3)\n"
    "v_mfma_f32_16x16x32_bf16 v[24:27], a[224:227], v[8:11], v[24:27]\n"
    "s_waitcnt lgkmcnt(2)\n"
    "v_mfma_f32_16x16x32_bf16 v[24:27], a[228:231], v[12:15], v[24:27]\n"
    "s_waitcnt lgkmcnt(1)\n"
    "v_mfma_f32_16x16x32_bf16 v[24:27], a[232:235], v[16:19], v[24:27]\n"
    "s_waitcnt lgkmcnt(0)\n"
    "v_mfma_f32_16x16x32_bf16 v[24:27], a[236:239], v[20:23], v[24:27]\n"
    "v_mfma_f32_16x16x32_bf16 v[28:31], a[240:243], v[0:3], 0\n"
    "v_mfma_f32_16x16x32_bf16 v[28:31], a[244:247], v[4:7], v[28:31]\n"
    "v_mfma_f32_16x16x32_bf16 v[28:31], a[248:251], v[8:11], v[28:31]\n"
    "v_mfma_f32_16x16x32_bf16 v[28:31], a[252:255], v[12:15], v[28:31]\n"
    "v_mfma_f32_16x16x32_bf16 v[28:31], v[224:227], v[16:19], v[28:31]\n"
    "v_mfma_f32_16x16x32_bf16 v[28:31], v[228:231], v[20:23], v[28:31]\n"
    "v_mfma_f32_16x16x32_bf16 v[32:35], v[232:235], v[0:3], 0\n"
    "v_mfma_f32_16x16x32_bf16 v[32:35], v[236:239], v[4:7], v[32:35]\n"
    "v_cvt_pk_bf16_f32 v0, v24, v25\n"
    "v_cvt_pk_bf16_f32 v1, v26, v27\n"
    "v_mfma_f32_16x16x32_bf16 v[32:35], v[240:243], v[8:11], v[32:35]\n"
    "v_mfma_f32_16x16x32_bf16 v[32:35], v[244:247], v[12:15], v[32:35]\n"
    "v_mfma_f32_16x16x32_bf16 v[32:35], v[248:251], v[16:19], v[32:35]\n"
    "v_mfma_f32_16x16x32_bf16 v[32:35], v[252:255], v[20:23], v[32:35]\n"
    "v_cvt_pk_bf16_f32 v2, v28, v29\n"
    "v_cvt_pk_bf16_f32 v3, v30, v31\n"
    "LPG3%=:\n"
    "s_cbranch_execz LWB%=\n"
    "buffer_store_dwordx4 v[0:3], v214, s[44:47], s87 offen offset:0 nt\n"
    "s_nop 0\n"
    "s_nop 0\n"
    "s_nop 0\n"
    "s_nop 0\n"
    "s_nop 0\n"
    "v_cvt_pk_bf16_f32 v0, v32, v33\n"
    "v_cvt_pk_bf16_f32 v1, v34, v35\n"
    "buffer_store_dwordx2 v[0:1], v215, s[44:47], s87 offen offset:0 nt\n"
    "LEND%=:\n"
    "s_endpgm\n"
    "LEXTRA%=:\n"
    "LPG4%=:\n"
    "s_cbranch_execz LWB%=\n"
    "s_waitcnt lgkmcnt(0)\n"
    "v_mfma_f32_16x16x32_bf16 v[136:139], v[36:39], v[64:67], v[136:139]\n"
    "ds_read_b128 v[0:3], v209 offset:8192\n"
    "v_mov_b64 v[4:5], v[84:85]\n"
    "v_mfma_f32_16x16x32_bf16 v[136:139], v[52:55], v[168:171], v[136:139]\n"
    "ds_read_b128 v[18:21], v210 offset:9216\n"
    "v_mov_b64 v[10:11], v[84:85]\n"
    "v_mfma_f32_16x16x32_bf16 v[140:143], v[40:43], v[64:67], v[140:143]\n"
    "ds_read_b128 v[22:25], v209 offset:10240\n"
    "v_mov_b64 v[16:17], v[84:85]\n"
    "v_mfma_f32_16x16x32_bf16 v[140:143], v[54:57], v[172:175], v[140:143]\n"
    "ds_read_b128 v[26:29], v210 offset:11264\n"
    "v_cvt_pk_bf16_f32 v110, v196, v197\n"
    "v_mfma_f32_16x16x32_bf16 v[144:147], v[44:47], v[64:67], v[144:147]\n"
    "ds_read_b128 v[6:9], v205 offset:28672\n"
    "v_max3_f32 v30, v136, v137, v138\n"
    "v_mfma_f32_16x16x32_bf16 v[144:147], v[58:61], v[174:177], v[144:147]\n"
    "ds_read_b128 v[12:15], v206 offset:28672\n"
    "v_cvt_pk_bf16_f32 v111, v198, v199\n"
    "v_mfma_f32_16x16x32_bf16 v[148:151], v[48:51], v[64:67], v[148:151]\n"
    "v_max3_f32 v31, v139, v140, v141\n"
    "v_max3_f32 v30, v30, v142, v143\n"
    "v_mfma_f32_16x16x32_bf16 v[148:151], v[60:63], v[178:181], v[148:151]\n"
    "ds_read_b128 v[32:35], v209 offset:12288\n"
    "v_mov_b64 v[112:113], v[86:87]\n"
    "v_mfma_f32_16x16x32_bf16 v[152:155], v[36:39], v[68:71], v[152:155]\n"
    "ds_read_b128 v[82:85], v210 offset:13312\n"
    "v_max3_f32 v31, v31, v144, v145\n"
    "v_mfma_f32_16x16x32_bf16 v[152:155], v[52:55], v[182:185], v[152:155]\n"
    "ds_read_b128 v[132:135], v209 offset:14336\n"
    "v_max3_f32 v30, v30, v146, v147\n"
    "v_mfma_f32_16x16x32_bf16 v[156:159], v[40:43], v[68:71], v[156:159]\n"
    "ds_read_b128 v[168:171], v210 offset:15360\n"
    "v_max3_f32 v31, v31, v148, v149\n"
    "v_mfma_f32_16x16x32_bf16 v[156:159], v[54:57], v[186:189], v[156:159]\n"
    "ds_read_b128 v[114:117], v205 offset:30720\n"
    "v_max3_f32 v30, v30, v150, v151\n"
    "v_mfma_f32_16x16x32_bf16 v[160:163], v[44:47], v[68:71], v[160:163]\n"
    "ds_read_b128 v[120:123], v206 offset:30720\n"
    "v_max3_f32 v126, v152, v153, v154\n"
    "v_mfma_f32_16x16x32_bf16 v[160:163], v[58:61], v[188:191], v[160:163]\n"
    "ds_read_b128 v[64:67], v207 offset:0\n"
    "v_max_f32 v30, v30, v31\n"
    "v_mfma_f32_16x16x32_bf16 v[164:167], v[48:51], v[68:71], v[164:167]\n"
    "ds_read_b128 v[68:71], v208 offset:0\n"
    "v_max3_f32 v31, v155, v156, v157\n"
    "v_mfma_f32_16x16x32_bf16 v[164:167], v[60:63], v[192:195], v[164:167]\n"
    "ds_read_b128 v[128:131], v207 offset:8192\n"
    "v_max3_f32 v126, v126, v158, v159\n"
    "s_waitcnt lgkmcnt(11)\n"
    "v_mfma_f32_16x16x32_bf16 v[0:3], v[36:39], v[72:75], v[0:3]\n"
    "ds_read_b128 v[172:175], v208 offset:8192\n"
    "v_max3_f32 v31, v31, v160, v161\n"
    "s_waitcnt lgkmcnt(11)\n"
    "v_mfma_f32_16x16x32_bf16 v[0:3], v[52:55], v[4:7], v[0:3]\n"
    "ds_read_b128 v[4:7], v207 offset:16384\n"
    "v_max3_f32 v126, v126, v162, v163\n"
    "v_mfma_f32_16x16x32_bf16 v[18:21], v[40:43], v[72:75], v[18:21]\n"
    "ds_read_b128 v[176:179], v208 offset:16384\n"
    "v_max3_f32 v31, v31, v164, v165\n"
    "v_mfma_f32_16x16x32_bf16 v[18:21], v[54:57], v[8:11], v[18:21]\n"
    "v_max3_f32 v126, v126, v166, v167\n"
    "v_mov_b64 v[118:119], v[86:87]\n"
    "v_mfma_f32_16x16x32_bf16 v[22:25], v[44:47], v[72:75], v[22:25]\n"
    "s_waitcnt lgkmcnt(10)\n"
    "v_max_f32 v126, v126, v31\n"
    "v_mov_b64 v[124:125], v[86:87]\n"
    "v_mfma_f32_16x16x32_bf16 v[22:25], v[58:61], v[10:13], v[22:25]\n"
    "v_permlane32_swap_b32 v30, v126\n"
    "v_mfma_f32_16x16x32_bf16 v[26:29], v[48:51], v[72:75], v[26:29]\n"
    "v_max_f32 v30, v30, v126\n"
    "v_xor_b32 v205, 0x8000, v205\n"
    "v_mfma_f32_16x16x32_bf16 v[26:29], v[60:63], v[14:17], v[26:29]\n"
    "v_mov_b32 v126, v30\n"
    "v_add_u32 v8, s64, v205\n"
    "v_mfma_f32_16x16x32_bf16 v[32:35], v[36:39], v[76:79], v[32:35]\n"
    "s_waitcnt lgkmcnt(6)\n"
    "v_permlane16_swap_b32 v30, v126\n"
    "v_mfma_f32_16x16x32_bf16 v[32:35], v[52:55], v[112:115], v[32:35]\n"
    "v_max_f32 v30, v30, v126\n"
    "v_xor_b32 v206, 0x8000, v206\n"
    "v_mfma_f32_16x16x32_bf16 v[82:85], v[40:43], v[76:79], v[82:85]\n"
    "v_add_u32 v9, s64, v206\n"
    "v_mul_f32 v30, s71, v30\n"
    "v_mfma_f32_16x16x32_bf16 v[82:85], v[54:57], v[116:119], v[82:85]\n"
    "v_mov_b32 v126, v30\n"
    "v_max3_f32 v10, v0, v1, v2\n"
    "v_mfma_f32_16x16x32_bf16 v[132:135], v[44:47], v[76:79], v[132:135]\n"
    "v_permlane32_swap_b32 v30, v126\n"
    "v_mfma_f32_16x16x32_bf16 v[132:135], v[58:61], v[118:121], v[132:135]\n"
    "ds_read_b128 v[12:15], v207 offset:2048\n"
    "v_max3_f32 v11, v32, v33, v34\n"
    "v_mfma_f32_16x16x32_bf16 v[168:171], v[48:51], v[76:79], v[168:171]\n"
    "ds_read_b128 v[36:39], v208 offset:2048\n"
    "v_max3_f32 v16, v3, v18, v19\n"
    "v_mfma_f32_16x16x32_bf16 v[168:171], v[60:63], v[122:125], v[168:171]\n"
    "ds_read_b128 v[40:43], v207 offset:10240\n"
    "v_max3_f32 v10, v10, v20, v21\n"
    "s_waitcnt lgkmcnt(3)\n"
    "v_mfma_f32_16x16x32_bf16 v[44:47], a[216:219], v[64:67], 0\n"
    "ds_read_b128 v[48:51], v208 offset:10240\n"
    "v_max3_f32 v17, v35, v82, v83\n"
    "v_mfma_f32_16x16x32_bf16 v[44:47], a[220:223], v[68:71], v[44:47]\n"
    "ds_read_b128 v[56:59], v207 offset:18432\n"
    "v_max3_f32 v11, v11, v84, v85\n"
    "v_mfma_f32_16x16x32_bf16 v[44:47], a[224:227], v[128:131], v[44:47]\n"
    "ds_read_b128 v[72:75], v208 offset:18432\n"
    "v_fma_f32 v136, v136, s68, v30\n"
    "v_mfma_f32_16x16x32_bf16 v[44:47], a[228:231], v[172:175], v[44:47]\n"
    "v_fma_f32 v137, v137, s68, v30\n"
    "v_fma_f32 v138, v138, s68, v30\n"
    "v_mfma_f32_16x16x32_bf16 v[44:47], a[232:235], v[4:7], v[44:47]\n"
    "v_fma_f32 v139, v139, s68, v30\n"
    "v_fma_f32 v140, v140, s68, v30\n"
    "v_mfma_f32_16x16x32_bf16 v[44:47], a[236:239], v[176:179], v[44:47]\n"
    "v_fma_f32 v141, v141, s68, v30\n"
    "v_fma_f32 v142, v142, s68, v30\n"
    "v_mfma_f32_16x16x32_bf16 v[76:79], a[240:243], v[64:67], 0\n"
    "v_fma_f32 v143, v143, s68, v30\n"
    "v_max3_f32 v16, v16, v22, v23\n"
    "v_mfma_f32_16x16x32_bf16 v[76:79], a[244:247], v[68:71], v[76:79]\n"
    "v_max3_f32 v10, v10, v24, v25\n"
    "v_max3_f32 v17, v17, v132, v133\n"
    "v_mfma_f32_16x16x32_bf16 v[76:79], a[248:251], v[128:131], v[76:79]\n"
    "v_cvt_pk_bf16_f32 v44, v44, v45\n"
    "v_cvt_pk_bf16_f32 v45, v46, v47\n"
    "v_mfma_f32_16x16x32_bf16 v[76:79], a[252:255], v[172:175], v[76:79]\n"
    "v_max3_f32 v11, v11, v134, v135\n"
    "v_max3_f32 v16, v16, v26, v27\n"
    "v_mfma_f32_16x16x32_bf16 v[76:79], v[224:227], v[4:7], v[76:79]\n"
    "v_exp_f32 v136, v136\n"
    "v_mfma_f32_16x16x32_bf16 v[76:79], v[228:231], v[176:179], v[76:79]\n"
    "v_exp_f32 v137, v137\n"
    "v_mfma_f32_16x16x32_bf16 v[112:115], v[232:235], v[64:67], 0\n"
    "v_exp_f32 v138, v138\n"
    "v_mfma_f32_16x16x32_bf16 v[112:115], v[236:239], v[68:71], v[112:115]\n"
    "v_exp_f32 v139, v139\n"
    "v_mfma_f32_16x16x32_bf16 v[112:115], v[240:243], v[128:131], v[112:115]\n"
    "v_exp_f32 v140, v140\n"
    "v_mfma_f32_16x16x32_bf16 v[112:115], v[244:247], v[172:175], v[112:115]\n"
    "ds_read_b128 v[62:65], v207 offset:4096\n"
    "v_cvt_pk_bf16_f32 v46, v76, v77\n"
    "v_mfma_f32_16x16x32_bf16 v[112:115], v[248:251], v[4:7], v[112:115]\n"
    "ds_read_b128 v[4:7], v208 offset:4096\n"
    "v_cvt_pk_bf16_f32 v47, v78, v79\n"
    "v_mfma_f32_16x16x32_bf16 v[112:115], v[252:255], v[176:179], v[112:115]\n"
    "buffer_store_dwordx4 v[44:47], v214, s[44:47], s80 offen offset:0 nt\n"
    "s_waitcnt lgkmcnt(2)\n"
    "v_mfma_f32_16x16x32_bf16 v[66:69], a[216:219], v[12:15], 0\n"
    "ds_read_b128 v[76:79], v207 offset:12288\n"
    "v_max3_f32 v10, v10, v28, v29\n"
    "v_mfma_f32_16x16x32_bf16 v[66:69], a[220:223], v[36:39], v[66:69]\n"
    "ds_read_b128 v[44:47], v208 offset:12288\n"
    "v_max3_f32 v17, v17, v168, v169\n"
    "v_mfma_f32_16x16x32_bf16 v[66:69], a[224:227], v[40:43], v[66:69]\n"
    "ds_read_b128 v[116:119], v207 offset:20480\n"
    "v_cvt_pk_bf16_f32 v52, v112, v113\n"
    "v_mfma_f32_16x16x32_bf16 v[66:69], a[228:231], v[48:51], v[66:69]\n"
    "ds_read_b128 v[120:123], v208 offset:20480\n"
    "v_cvt_pk_bf16_f32 v53, v114, v115\n"
    "v_mfma_f32_16x16x32_bf16 v[66:69], a[232:235], v[56:59], v[66:69]\n"
    "buffer_store_dwordx2 v[52:53], v215, s[44:47], s80 offen offset:0 nt\n"
    "v_mfma_f32_16x16x32_bf16 v[66:69], a[236:239], v[72:75], v[66:69]\n"
    "v_exp_f32 v141, v141\n"
    "v_mfma_f32_16x16x32_bf16 v[112:115], a[240:243], v[12:15], 0\n"
    "v_exp_f32 v142, v142\n"
    "v_mfma_f32_16x16x32_bf16 v[112:115], a[244:247], v[36:39], v[112:115]\n"
    "v_exp_f32 v143, v143\n"
    "v_mfma_f32_16x16x32_bf16 v[112:115], a[248:251], v[40:43], v[112:115]\n"
    "v_max3_f32 v11, v11, v170, v171\n"
    "v_max_f32 v10, v10, v16\n"
    "v_mfma_f32_16x16x32_bf16 v[112:115], a[252:255], v[48:51], v[112:115]\n"
    "v_cvt_pk_bf16_f32 v66, v66, v67\n"
    "v_cvt_pk_bf16_f32 v67, v68, v69\n"
    "v_mfma_f32_16x16x32_bf16 v[112:115], v[224:227], v[56:59], v[112:115]\n"
    "v_max_f32 v11, v11, v17\n"
    "v_fma_f32 v144, v144, s68, v30\n"
    "v_mfma_f32_16x16x32_bf16 v[112:115], v[228:231], v[72:75], v[112:115]\n"
    "ds_read_b128 v[128:131], v207 offset:6144\n"
    "v_fma_f32 v145, v145, s68, v30\n"
    "v_mfma_f32_16x16x32_bf16 v[172:175], v[232:235], v[12:15], 0\n"
    "ds_read_b128 v[12:15], v208 offset:6144\n"
    "v_fma_f32 v146, v146, s68, v30\n"
    "v_mfma_f32_16x16x32_bf16 v[172:175], v[236:239], v[36:39], v[172:175]\n"
    "ds_read_b128 v[36:39], v207 offset:14336\n"
    "v_fma_f32 v147, v147, s68, v30\n"
    "v_mfma_f32_16x16x32_bf16 v[172:175], v[240:243], v[40:43], v[172:175]\n"
    "ds_read_b128 v[40:43], v208 offset:14336\n"
    "v_cvt_pk_bf16_f32 v68, v112, v113\n"
    "v_mfma_f32_16x16x32_bf16 v[172:175], v[244:247], v[48:51], v[172:175]\n"
    "ds_read_b128 v[48:51], v207 offset:22528\n"
    "v_cvt_pk_bf16_f32 v69, v114, v115\n"
    "v_mfma_f32_16x16x32_bf16 v[172:175], v[248:251], v[56:59], v[172:175]\n"
    "buffer_store_dwordx4 v[66:69], v214, s[44:47], s81 offen offset:0 nt\n"
    "v_mfma_f32_16x16x32_bf16 v[172:175], v[252:255], v[72:75], v[172:175]\n"
    "ds_read_b128 v[56:59], v208 offset:22528\n"
    "v_fma_f32 v148, v148, s68, v30\n"
    "s_waitcnt lgkmcnt(6)\n"
    "v_mfma_f32_16x16x32_bf16 v[66:69], a[216:219], v[62:65], 0\n"
    "v_fma_f32 v149, v149, s68, v30\n"
    "v_fma_f32 v150, v150, s68, v30\n"
    "v_mfma_f32_16x16x32_bf16 v[66:69], a[220:223], v[4:7], v[66:69]\n"
    "v_fma_f32 v151, v151, s68, v30\n"
    "v_cvt_pk_bf16_f32 v70, v136, v137\n"
    "v_mfma_f32_16x16x32_bf16 v[66:69], a[224:227], v[76:79], v[66:69]\n"
    "v_cvt_pk_bf16_f32 v16, v172, v173\n"
    "v_cvt_pk_bf16_f32 v17, v174, v175\n"
    "v_mfma_f32_16x16x32_bf16 v[66:69], a[228:231], v[44:47], v[66:69]\n"
    "buffer_store_dwordx2 v[16:17], v215, s[44:47], s81 offen offset:0 nt\n"
    "v_mfma_f32_16x16x32_bf16 v[66:69], a[232:235], v[116:119], v[66:69]\n"
    "v_cvt_pk_bf16_f32 v71, v138, v139\n"
    "v_cvt_pk_bf16_f32 v72, v140, v141\n"
    "v_mfma_f32_16x16x32_bf16 v[66:69], a[236:239], v[120:123], v[66:69]\n"
    "v_cvt_pk_bf16_f32 v73, v142, v143\n"
    "v_fma_f32 v152, v152, s68, v126\n"
    "v_mfma_f32_16x16x32_bf16 v[112:115], a[240:243], v[62:65], 0\n"
    "v_exp_f32 v144, v144\n"
    "v_mfma_f32_16x16x32_bf16 v[136:139], v[220:223], v[70:73], 0\n"
    "v_exp_f32 v145, v145\n"
    "v_mfma_f32_16x16x32_bf16 v[112:115], a[244:247], v[4:7], v[112:115]\n"
    "v_exp_f32 v146, v146\n"
    "v_mfma_f32_16x16x32_bf16 v[112:115], a[248:251], v[76:79], v[112:115]\n"
    "v_cvt_pk_bf16_f32 v66, v66, v67\n"
    "v_cvt_pk_bf16_f32 v67, v68, v69\n"
    "v_mfma_f32_16x16x32_bf16 v[112:115], a[252:255], v[44:47], v[112:115]\n"
    "v_exp_f32 v147, v147\n"
    "v_mfma_f32_16x16x32_bf16 v[112:115], v[224:227], v[116:119], v[112:115]\n"
    "v_exp_f32 v148, v148\n"
    "v_mfma_f32_16x16x32_bf16 v[112:115], v[228:231], v[120:123], v[112:115]\n"
    "v_exp_f32 v149, v149\n"
    "v_mfma_f32_16x16x32_bf16 v[140:143], v[232:235], v[62:65], 0\n"
    "v_exp_f32 v150, v150\n"
    "v_mfma_f32_16x16x32_bf16 v[140:143], v[236:239], v[4:7], v[140:143]\n"
    "v_exp_f32 v151, v151\n"
    "v_mfma_f32_16x16x32_bf16 v[140:143], v[240:243], v[76:79], v[140:143]\n"
    "v_permlane32_swap_b32 v10, v11\n"
    "v_mfma_f32_16x16x32_bf16 v[140:143], v[244:247], v[44:47], v[140:143]\n"
    "v_cvt_pk_bf16_f32 v68, v112, v113\n"
    "v_cvt_pk_bf16_f32 v69, v114, v115\n"
    "v_mfma_f32_16x16x32_bf16 v[140:143], v[248:251], v[116:119], v[140:143]\n"
    "buffer_store_dwordx4 v[66:69], v214, s[44:47], s82 offen offset:0 nt\n"
    "v_mfma_f32_16x16x32_bf16 v[140:143], v[252:255], v[120:123], v[140:143]\n"
    "s_waitcnt lgkmcnt(0)\n"
    "v_cvt_pk_bf16_f32 v4, v144, v145\n"
    "v_cvt_pk_bf16_f32 v5, v146, v147\n"
    "v_mfma_f32_16x16x32_bf16 v[44:47], a[216:219], v[128:131], 0\n"
    "v_cvt_pk_bf16_f32 v6, v148, v149\n"
    "v_cvt_pk_bf16_f32 v7, v150, v151\n"
    "v_mfma_f32_16x16x32_bf16 v[44:47], a[220:223], v[12:15], v[44:47]\n"
    "v_max_f32 v10, v10, v11\n"
    "v_fma_f32 v153, v153, s68, v126\n"
    "v_mfma_f32_16x16x32_bf16 v[136:139], v[220:223], v[4:7], v[136:139]\n"
    "v_cvt_pk_bf16_f32 v16, v140, v141\n"
    "v_cvt_pk_bf16_f32 v17, v142, v143\n"
    "v_mfma_f32_16x16x32_bf16 v[62:65], v[88:91], v[70:73], 0\n"
    "buffer_store_dwordx2 v[16:17], v215, s[44:47], s82 offen offset:0 nt\n"
    "v_mfma_f32_16x16x32_bf16 v[62:65], v[92:95], v[4:7], v[62:65]\n"
    "v_mov_b32 v11, v10\n"
    "v_fma_f32 v154, v154, s68, v126\n"
    "v_mfma_f32_16x16x32_bf16 v[66:69], v[96:99], v[70:73], 0\n"
    "v_rcp_f32 v16, v136\n"
    "v_mfma_f32_16x16x32_bf16 v[66:69], v[100:103], v[4:7], v[66:69]\n"
    "v_permlane16_swap_b32 v10, v11\n"
    "v_mfma_f32_16x16x32_bf16 v[74:77], v[104:107], v[70:73], 0\n"
    "v_fma_f32 v155, v155, s68, v126\n"
    "v_mul_f32 v62, v62, v16\n"
    "v_mfma_f32_16x16x32_bf16 v[74:77], v[108:111], v[4:7], v[74:77]\n"
    "v_mul_f32 v63, v63, v16\n"
    "v_mul_f32 v64, v64, v16\n"
    "v_mfma_f32_16x16x32_bf16 v[44:47], a[224:227], v[36:39], v[44:47]\n"
    "v_mul_f32 v65, v65, v16\n"
    "v_mul_f32 v66, v66, v16\n"
    "v_mfma_f32_16x16x32_bf16 v[44:47], a[228:231], v[40:43], v[44:47]\n"
    "v_mul_f32 v67, v67, v16\n"
    "v_mul_f32 v68, v68, v16\n"
    "v_mfma_f32_16x16x32_bf16 v[44:47], a[232:235], v[48:51], v[44:47]\n"
    "v_mul_f32 v69, v69, v16\n"
    "v_mul_f32 v74, v74, v16\n"
    "v_mfma_f32_16x16x32_bf16 v[44:47], a[236:239], v[56:59], v[44:47]\n"
    "s_barrier\n"
    "v_mfma_f32_16x16x32_bf16 v[4:7], a[240:243], v[128:131], 0\n"
    "ds_read_b128 v[70:73], v8 offset:0\n"
    "v_mul_f32 v75, v75, v16\n"
    "v_mfma_f32_16x16x32_bf16 v[4:7], a[244:247], v[12:15], v[4:7]\n"
    "ds_read_b128 v[112:115], v9 offset:0\n"
    "v_mul_f32 v76, v76, v16\n"
    "v_mfma_f32_16x16x32_bf16 v[4:7], a[248:251], v[36:39], v[4:7]\n"
    "ds_read_b128 v[116:119], v8 offset:8192\n"
    "v_cvt_pk_bf16_f32 v44, v44, v45\n"
    "v_mfma_f32_16x16x32_bf16 v[4:7], a[252:255], v[40:43], v[4:7]\n"
    "ds_read_b128 v[120:123], v9 offset:8192\n"
    "v_cvt_pk_bf16_f32 v45, v46, v47\n"
    "v_mfma_f32_16x16x32_bf16 v[4:7], v[224:227], v[48:51], v[4:7]\n"
    "ds_read_b128 v[136:139], v8 offset:16384\n"
    "v_mul_f32 v77, v77, v16\n"
    "v_mfma_f32_16x16x32_bf16 v[4:7], v[228:231], v[56:59], v[4:7]\n"
    "ds_read_b128 v[140:143], v9 offset:16384\n"
    "v_cvt_pk_bf16_f32 v16, v62, v63\n"
    "v_mfma_f32_16x16x32_bf16 v[144:147], v[232:235], v[128:131], 0\n"
    "v_cvt_pk_bf16_f32 v17, v64, v65\n"
    "v_cvt_pk_bf16_f32 v30, v66, v67\n"
    "v_mfma_f32_16x16x32_bf16 v[144:147], v[236:239], v[12:15], v[144:147]\n"
    "ds_write_b64 v211, v[16:17] offset:0\n"
    "v_mfma_f32_16x16x32_bf16 v[144:147], v[240:243], v[36:39], v[144:147]\n"
    "v_cvt_pk_bf16_f32 v46, v4, v5\n"
    "v_cvt_pk_bf16_f32 v47, v6, v7\n"
    "v_mfma_f32_16x16x32_bf16 v[144:147], v[244:247], v[40:43], v[144:147]\n"
    "ds_read_b128 v[4:7], v205 offset:0\n"
    "v_cvt_pk_bf16_f32 v31, v68, v69\n"
    "v_mfma_f32_16x16x32_bf16 v[144:147], v[248:251], v[48:51], v[144:147]\n"
    "ds_write_b64 v212, v[30:31] offset:0\n"
    "v_mfma_f32_16x16x32_bf16 v[144:147], v[252:255], v[56:59], v[144:147]\n"
    "ds_read_b128 v[12:15], v206 offset:0\n"
    "v_cvt_pk_bf16_f32 v16, v74, v75\n"
    "s_waitcnt lgkmcnt(9)\n"
    "v_mfma_f32_16x16x32_bf16 v[36:39], a[72:75], v[70:73], 0\n"
    "ds_read_b128 v[40:43], v205 offset:8192\n"
    "v_cvt_pk_bf16_f32 v17, v76, v77\n"
    "s_waitcnt lgkmcnt(9)\n"
    "v_mfma_f32_16x16x32_bf16 v[36:39], a[76:79], v[112:115], v[36:39]\n"
    "ds_write_b64 v213, v[16:17] offset:0\n"
    "s_waitcnt lgkmcnt(9)\n"
    "v_mfma_f32_16x16x32_bf16 v[36:39], a[80:83], v[116:119], v[36:39]\n"
    "ds_read_b128 v[48:51], v206 offset:8192\n"
    "v_cvt_pk_bf16_f32 v16, v144, v145\n"
    "s_waitcnt lgkmcnt(9)\n"
    "v_mfma_f32_16x16x32_bf16 v[36:39], a[84:87], v[120:123], v[36:39]\n"
    "ds_read_b128 v[56:59], v205 offset:16384\n"
    "v_cvt_pk_bf16_f32 v17, v146, v147\n"
    "s_waitcnt lgkmcnt(9)\n"
    "v_mfma_f32_16x16x32_bf16 v[36:39], a[88:91], v[136:139], v[36:39]\n"
    "buffer_store_dwordx4 v[44:47], v214, s[44:47], s83 offen offset:0 nt\n"
    "s_waitcnt lgkmcnt(8)\n"
    "v_mfma_f32_16x16x32_bf16 v[36:39], a[92:95], v[140:143], v[36:39]\n"
    "ds_read_b128 v[74:77], v206 offset:16384\n"
    "v_fma_f32 v156, v156, s68, v126\n"
    "v_mfma_f32_16x16x32_bf16 v[44:47], a[96:99], v[70:73], 0\n"
    "v_fma_f32 v157, v157, s68, v126\n"
    "v_fma_f32 v158, v158, s68, v126\n"
    "v_mfma_f32_16x16x32_bf16 v[44:47], a[100:103], v[112:115], v[44:47]\n"
    "buffer_store_dwordx2 v[16:17], v215, s[44:47], s83 offen offset:0 nt\n"
    "v_mfma_f32_16x16x32_bf16 v[44:47], a[104:107], v[116:119], v[44:47]\n"
    "v_cvt_pk_bf16_f32 v64, v36, v37\n"
    "v_cvt_pk_bf16_f32 v65, v38, v39\n"
    "v_mfma_f32_16x16x32_bf16 v[44:47], a[108:111], v[120:123], v[44:47]\n"
    "v_fma_f32 v159, v159, s68, v126\n"
    "v_fma_f32 v160, v160, s68, v126\n"
    "v_mfma_f32_16x16x32_bf16 v[44:47], a[112:115], v[136:139], v[44:47]\n"
    "v_exp_f32 v152, v152\n"
    "v_mfma_f32_16x16x32_bf16 v[44:47], a[116:119], v[140:143], v[44:47]\n"
    "v_exp_f32 v153, v153\n"
    "v_mfma_f32_16x16x32_bf16 v[36:39], a[120:123], v[70:73], 0\n"
    "v_exp_f32 v154, v154\n"
    "v_mfma_f32_16x16x32_bf16 v[36:39], a[124:127], v[112:115], v[36:39]\n"
    "v_exp_f32 v155, v155\n"
    "v_mfma_f32_16x16x32_bf16 v[36:39], a[128:131], v[116:119], v[36:39]\n"
    "v_exp_f32 v156, v156\n"
    "v_mfma_f32_16x16x32_bf16 v[36:39], a[132:135], v[120:123], v[36:39]\n"
    "v_cvt_pk_bf16_f32 v66, v44, v45\n"
    "v_cvt_pk_bf16_f32 v67, v46, v47\n"
    "v_mfma_f32_16x16x32_bf16 v[36:39], a[136:139], v[136:139], v[36:39]\n"
    "v_exp_f32 v157, v157\n"
    "v_mfma_f32_16x16x32_bf16 v[36:39], a[140:143], v[140:143], v[36:39]\n"
    "s_waitcnt lgkmcnt(0)\n"
    "v_exp_f32 v158, v158\n"
    "v_mfma_f32_16x16x32_bf16 v[44:47], a[0:3], v[4:7], 0\n"
    "v_exp_f32 v159, v159\n"
    "v_mfma_f32_16x16x32_bf16 v[44:47], a[4:7], v[12:15], v[44:47]\n"
    "v_fma_f32 v161, v161, s68, v126\n"
    "v_fma_f32 v162, v162, s68, v126\n"
    "v_mfma_f32_16x16x32_bf16 v[44:47], a[8:11], v[40:43], v[44:47]\n"
    "v_fma_f32 v163, v163, s68, v126\n"
    "v_cvt_pk_bf16_f32 v80, v36, v37\n"
    "v_mfma_f32_16x16x32_bf16 v[44:47], a[12:15], v[48:51], v[44:47]\n"
    "v_cvt_pk_bf16_f32 v81, v38, v39\n"
    "v_fma_f32 v164, v164, s68, v126\n"
    "v_mfma_f32_16x16x32_bf16 v[44:47], a[16:19], v[56:59], v[44:47]\n"
    "v_fma_f32 v165, v165, s68, v126\n"
    "v_fma_f32 v166, v166, s68, v126\n"
    "v_mfma_f32_16x16x32_bf16 v[44:47], a[20:23], v[74:77], v[44:47]\n"
    "v_fma_f32 v167, v167, s68, v126\n"
    "v_cvt_pk_bf16_f32 v68, v152, v153\n"
    "v_mfma_f32_16x16x32_bf16 v[112:115], a[24:27], v[4:7], 0\n"
    "v_cvt_pk_bf16_f32 v69, v154, v155\n"
    "v_cvt_pk_bf16_f32 v70, v156, v157\n"
    "v_mfma_f32_16x16x32_bf16 v[112:115], a[28:31], v[12:15], v[112:115]\n"
    "v_cvt_pk_bf16_f32 v71, v158, v159\n"
    "v_max_f32 v10, v10, v11\n"
    "v_mfma_f32_16x16x32_bf16 v[112:115], a[32:35], v[40:43], v[112:115]\n"
    "v_exp_f32 v160, v160\n"
    "v_mfma_f32_16x16x32_bf16 v[116:119], v[220:223], v[68:71], 0\n"
    "v_exp_f32 v161, v161\n"
    "v_mfma_f32_16x16x32_bf16 v[112:115], a[36:39], v[48:51], v[112:115]\n"
    "v_exp_f32 v162, v162\n"
    "v_mfma_f32_16x16x32_bf16 v[112:115], a[40:43], v[56:59], v[112:115]\n"
    "v_exp_f32 v163, v163\n"
    "v_mfma_f32_16x16x32_bf16 v[112:115], a[44:47], v[74:77], v[112:115]\n"
    "v_exp_f32 v164, v164\n"
    "v_mfma_f32_16x16x32_bf16 v[120:123], a[48:51], v[4:7], 0\n"
    "v_exp_f32 v165, v165\n"
    "v_mfma_f32_16x16x32_bf16 v[120:123], a[52:55], v[12:15], v[120:123]\n"
    "v_exp_f32 v166, v166\n"
    "v_mfma_f32_16x16x32_bf16 v[120:123], a[56:59], v[40:43], v[120:123]\n"
    "v_exp_f32 v167, v167\n"
    "v_mfma_f32_16x16x32_bf16 v[120:123], a[60:63], v[48:51], v[120:123]\n"
    "ds_read_b128 v[124:127], v205 offset:2048\n"
    "v_cvt_pk_bf16_f32 v36, v44, v45\n"
    "v_mfma_f32_16x16x32_bf16 v[120:123], a[64:67], v[56:59], v[120:123]\n"
    "ds_read_b128 v[136:139], v206 offset:2048\n"
    "v_cvt_pk_bf16_f32 v37, v46, v47\n"
    "v_mfma_f32_16x16x32_bf16 v[120:123], a[68:71], v[74:77], v[120:123]\n"
    "ds_read_b128 v[44:47], v205 offset:10240\n"
    "v_cvt_pk_bf16_f32 v128, v160, v161\n"
    "v_mfma_f32_16x16x32_bf16 v[140:143], v[4:7], a[144:147], 0\n"
    "ds_read_b128 v[144:147], v206 offset:10240\n"
    "v_cvt_pk_bf16_f32 v129, v162, v163\n"
    "v_mfma_f32_16x16x32_bf16 v[140:143], v[12:15], a[148:151], v[140:143]\n"
    "ds_read_b128 v[148:151], v205 offset:18432\n"
    "v_cvt_pk_bf16_f32 v130, v164, v165\n"
    "v_mfma_f32_16x16x32_bf16 v[140:143], v[40:43], a[152:155], v[140:143]\n"
    "ds_read_b128 v[152:155], v206 offset:18432\n"
    "v_cvt_pk_bf16_f32 v131, v166, v167\n"
    "v_mfma_f32_16x16x32_bf16 v[140:143], v[48:51], a[156:159], v[140:143]\n"
    "v_cvt_pk_bf16_f32 v38, v112, v113\n"
    "v_cvt_pk_bf16_f32 v39, v114, v115\n"
    "v_mfma_f32_16x16x32_bf16 v[116:119], v[220:223], v[128:131], v[116:119]\n"
    "v_mul_f32 v10, s71, v10\n"
    "v_cvt_pk_bf16_f32 v52, v120, v121\n"
    "v_mfma_f32_16x16x32_bf16 v[112:115], v[88:91], v[68:71], 0\n"
    "v_cvt_pk_bf16_f32 v53, v122, v123\n"
    "v_mov_b32 v11, v10\n"
    "v_mfma_f32_16x16x32_bf16 v[112:115], v[92:95], v[128:131], v[112:115]\n"
    "v_lshl_add_u32 v16, s64, 1, v209\n"
    "v_lshl_add_u32 v17, s64, 1, v210\n"
    "v_mfma_f32_16x16x32_bf16 v[118:121], v[96:99], v[68:71], 0\n"
    "v_rcp_f32 v30, v116\n"
    "v_mfma_f32_16x16x32_bf16 v[118:121], v[100:103], v[128:131], v[118:121]\n"
    "v_permlane32_swap_b32 v10, v11\n"
    "v_mfma_f32_16x16x32_bf16 v[156:159], v[104:107], v[68:71], 0\n"
    "v_fma_f32 v0, v0, s68, v10\n"
    "v_mul_f32 v112, v112, v30\n"
    "v_mfma_f32_16x16x32_bf16 v[156:159], v[108:111], v[128:131], v[156:159]\n"
    "v_mul_f32 v113, v113, v30\n"
    "v_mul_f32 v114, v114, v30\n"
    "v_mfma_f32_16x16x32_bf16 v[140:143], v[56:59], a[160:163], v[140:143]\n"
    "v_mul_f32 v115, v115, v30\n"
    "v_mul_f32 v118, v118, v30\n"
    "v_mfma_f32_16x16x32_bf16 v[140:143], v[74:77], a[164:167], v[140:143]\n"
    "v_mul_f32 v119, v119, v30\n"
    "v_mul_f32 v120, v120, v30\n"
    "v_mfma_f32_16x16x32_bf16 v[68:71], v[4:7], a[168:171], 0\n"
    "v_mul_f32 v121, v121, v30\n"
    "v_mul_f32 v156, v156, v30\n"
    "v_mfma_f32_16x16x32_bf16 v[68:71], v[12:15], a[172:175], v[68:71]\n"
    "v_mul_f32 v157, v157, v30\n"
    "v_mul_f32 v158, v158, v30\n"
    "v_mfma_f32_16x16x32_bf16 v[68:71], v[40:43], a[176:179], v[68:71]\n"
    "v_mul_f32 v159, v159, v30\n"
    "v_cvt_pk_bf16_f32 v30, v112, v113\n"
    "v_mfma_f32_16x16x32_bf16 v[68:71], v[48:51], a[180:183], v[68:71]\n"
    "v_cvt_pk_bf16_f32 v31, v114, v115\n"
    "v_cvt_pk_bf16_f32 v62, v118, v119\n"
    "v_mfma_f32_16x16x32_bf16 v[68:71], v[56:59], a[184:187], v[68:71]\n"
    "ds_write_b64 v211, v[30:31] offset:2048\n"
    "v_mfma_f32_16x16x32_bf16 v[68:71], v[74:77], a[188:191], v[68:71]\n"
    "v_cvt_pk_bf16_f32 v63, v120, v121\n"
    "v_cvt_pk_bf16_f32 v30, v156, v157\n"
    "v_mfma_f32_16x16x32_bf16 v[114:117], v[4:7], a[192:195], 0\n"
    "v_cvt_pk_bf16_f32 v31, v158, v159\n"
    "v_cvt_pk_bf16_f32 v112, v140, v141\n"
    "v_mfma_f32_16x16x32_bf16 v[114:117], v[12:15], a[196:199], v[114:117]\n"
    "ds_write_b64 v212, v[62:63] offset:2048\n"
    "v_mfma_f32_16x16x32_bf16 v[114:117], v[40:43], a[200:203], v[114:117]\n"
    "v_cvt_pk_bf16_f32 v113, v142, v143\n"
    "v_cvt_pk_bf16_f32 v120, v68, v69\n"
    "v_mfma_f32_16x16x32_bf16 v[114:117], v[48:51], a[204:207], v[114:117]\n"
    "v_cvt_pk_bf16_f32 v121, v70, v71\n"
    "v_fma_f32 v1, v1, s68, v10\n"
    "v_mfma_f32_16x16x32_bf16 v[114:117], v[56:59], a[208:211], v[114:117]\n"
    "ds_write_b64 v213, v[30:31] offset:2048\n"
    "v_mfma_f32_16x16x32_bf16 v[114:117], v[74:77], a[212:215], v[114:117]\n"
    "s_waitcnt lgkmcnt(3)\n"
    "v_fma_f32 v2, v2, s68, v10\n"
    "v_fma_f32 v3, v3, s68, v10\n"
    "v_mfma_f32_16x16x32_bf16 v[4:7], a[0:3], v[124:127], 0\n"
    "v_fma_f32 v18, v18, s68, v10\n"
    "v_fma_f32 v19, v19, s68, v10\n"
    "v_mfma_f32_16x16x32_bf16 v[4:7], a[4:7], v[136:139], v[4:7]\n"
    "v_fma_f32 v20, v20, s68, v10\n"
    "v_fma_f32 v21, v21, s68, v10\n"
    "v_mfma_f32_16x16x32_bf16 v[4:7], a[8:11], v[44:47], v[4:7]\n"
    "v_cvt_pk_bf16_f32 v128, v114, v115\n"
    "v_cvt_pk_bf16_f32 v129, v116, v117\n"
    "v_mfma_f32_16x16x32_bf16 v[4:7], a[12:15], v[144:147], v[4:7]\n"
    "v_exp_f32 v0, v0\n"
    "v_mfma_f32_16x16x32_bf16 v[4:7], a[16:19], v[148:151], v[4:7]\n"
    "v_exp_f32 v1, v1\n"
    "v_mfma_f32_16x16x32_bf16 v[4:7], a[20:23], v[152:155], v[4:7]\n"
    "v_exp_f32 v2, v2\n"
    "v_mfma_f32_16x16x32_bf16 v[12:15], a[24:27], v[124:127], 0\n"
    "v_exp_f32 v3, v3\n"
    "v_mfma_f32_16x16x32_bf16 v[12:15], a[28:31], v[136:139], v[12:15]\n"
    "v_exp_f32 v18, v18\n"
    "v_mfma_f32_16x16x32_bf16 v[12:15], a[32:35], v[44:47], v[12:15]\n"
    "v_exp_f32 v19, v19\n"
    "v_mfma_f32_16x16x32_bf16 v[12:15], a[36:39], v[144:147], v[12:15]\n"
    "v_exp_f32 v20, v20\n"
    "v_mfma_f32_16x16x32_bf16 v[12:15], a[40:43], v[148:151], v[12:15]\n"
    "v_exp_f32 v21, v21\n"
    "v_mfma_f32_16x16x32_bf16 v[12:15], a[44:47], v[152:155], v[12:15]\n"
    "v_fma_f32 v22, v22, s68, v10\n"
    "v_fma_f32 v23, v23, s68, v10\n"
    "v_mfma_f32_16x16x32_bf16 v[48:51], a[48:51], v[124:127], 0\n"
    "v_fma_f32 v24, v24, s68, v10\n"
    "v_fma_f32 v25, v25, s68, v10\n"
    "v_mfma_f32_16x16x32_bf16 v[48:51], a[52:55], v[136:139], v[48:51]\n"
    "v_fma_f32 v26, v26, s68, v10\n"
    "v_fma_f32 v27, v27, s68, v10\n"
    "v_mfma_f32_16x16x32_bf16 v[48:51], a[56:59], v[44:47], v[48:51]\n"
    "v_fma_f32 v28, v28, s68, v10\n"
    "v_fma_f32 v29, v29, s68, v10\n"
    "v_mfma_f32_16x16x32_bf16 v[48:51], a[60:63], v[144:147], v[48:51]\n"
    "ds_read_b128 v[68:71], v205 offset:4096\n"
    "v_cvt_pk_bf16_f32 v0, v0, v1\n"
    "v_mfma_f32_16x16x32_bf16 v[48:51], a[64:67], v[148:151], v[48:51]\n"
    "ds_read_b128 v[72:75], v206 offset:4096\n"
    "v_cvt_pk_bf16_f32 v1, v2, v3\n"
    "v_mfma_f32_16x16x32_bf16 v[48:51], a[68:71], v[152:155], v[48:51]\n"
    "ds_read_b128 v[76:79], v205 offset:12288\n"
    "v_cvt_pk_bf16_f32 v2, v18, v19\n"
    "v_mfma_f32_16x16x32_bf16 v[114:117], v[124:127], a[144:147], 0\n"
    "ds_read_b128 v[140:143], v206 offset:12288\n"
    "v_cvt_pk_bf16_f32 v3, v20, v21\n"
    "v_mfma_f32_16x16x32_bf16 v[114:117], v[136:139], a[148:151], v[114:117]\n"
    "ds_read_b128 v[18:21], v205 offset:20480\n"
    "v_cvt_pk_bf16_f32 v40, v4, v5\n"
    "v_mfma_f32_16x16x32_bf16 v[156:159], v[220:223], v[0:3], 0\n"
    "v_exp_f32 v22, v22\n"
    "v_mfma_f32_16x16x32_bf16 v[114:117], v[44:47], a[152:155], v[114:117]\n"
    "ds_read_b128 v[160:163], v206 offset:20480\n"
    "v_cvt_pk_bf16_f32 v41, v6, v7\n"
    "v_mfma_f32_16x16x32_bf16 v[114:117], v[144:147], a[156:159], v[114:117]\n"
    "v_exp_f32 v23, v23\n"
    "v_mfma_f32_16x16x32_bf16 v[114:117], v[148:151], a[160:163], v[114:117]\n"
    "v_exp_f32 v24, v24\n"
    "v_mfma_f32_16x16x32_bf16 v[114:117], v[152:155], a[164:167], v[114:117]\n"
    "v_exp_f32 v25, v25\n"
    "v_mfma_f32_16x16x32_bf16 v[4:7], v[124:127], a[168:171], 0\n"
    "v_exp_f32 v26, v26\n"
    "v_mfma_f32_16x16x32_bf16 v[4:7], v[136:139], a[172:175], v[4:7]\n"
    "v_exp_f32 v27, v27\n"
    "v_mfma_f32_16x16x32_bf16 v[4:7], v[44:47], a[176:179], v[4:7]\n"
    "v_exp_f32 v28, v28\n"
    "v_mfma_f32_16x16x32_bf16 v[4:7], v[144:147], a[180:183], v[4:7]\n"
    "v_exp_f32 v29, v29\n"
    "v_mfma_f32_16x16x32_bf16 v[4:7], v[148:151], a[184:187], v[4:7]\n"
    "v_cvt_pk_bf16_f32 v22, v22, v23\n"
    "v_cvt_pk_bf16_f32 v23, v24, v25\n"
    "v_mfma_f32_16x16x32_bf16 v[4:7], v[152:155], a[188:191], v[4:7]\n"
    "v_cvt_pk_bf16_f32 v24, v26, v27\n"
    "v_cvt_pk_bf16_f32 v25, v28, v29\n"
    "v_mfma_f32_16x16x32_bf16 v[26:29], v[124:127], a[192:195], 0\n"
    "v_cvt_pk_bf16_f32 v42, v12, v13\n"
    "v_cvt_pk_bf16_f32 v43, v14, v15\n"
    "LPG5%=:\n"
    "s_cbranch_execz LWB%=\n"
    "v_mfma_f32_16x16x32_bf16 v[156:159], v[220:223], v[22:25], v[156:159]\n"
    "v_cvt_pk_bf16_f32 v56, v48, v49\n"
    "v_cvt_pk_bf16_f32 v57, v50, v51\n"
    "v_mfma_f32_16x16x32_bf16 v[12:15], v[88:91], v[0:3], 0\n"
    "v_cvt_pk_bf16_f32 v114, v114, v115\n"
    "v_cvt_pk_bf16_f32 v115, v116, v117\n"
    "v_mfma_f32_16x16x32_bf16 v[12:15], v[92:95], v[22:25], v[12:15]\n"
    "v_cvt_pk_bf16_f32 v122, v4, v5\n"
    "v_cvt_pk_bf16_f32 v123, v6, v7\n"
    "v_mfma_f32_16x16x32_bf16 v[4:7], v[96:99], v[0:3], 0\n"
    "v_rcp_f32 v10, v156\n"
    "v_mfma_f32_16x16x32_bf16 v[4:7], v[100:103], v[22:25], v[4:7]\n"
    "v_fma_f32 v32, v32, s68, v11\n"
    "v_fma_f32 v33, v33, s68, v11\n"
    "v_mfma_f32_16x16x32_bf16 v[48:51], v[104:107], v[0:3], 0\n"
    "v_mul_f32 v12, v12, v10\n"
    "v_mul_f32 v13, v13, v10\n"
    "v_mfma_f32_16x16x32_bf16 v[48:51], v[108:111], v[22:25], v[48:51]\n"
    "v_mul_f32 v14, v14, v10\n"
    "v_mul_f32 v15, v15, v10\n"
    "v_mfma_f32_16x16x32_bf16 v[26:29], v[136:139], a[196:199], v[26:29]\n"
    "v_mul_f32 v4, v4, v10\n"
    "v_mul_f32 v5, v5, v10\n"
    "v_mfma_f32_16x16x32_bf16 v[26:29], v[44:47], a[200:203], v[26:29]\n"
    "v_mul_f32 v6, v6, v10\n"
    "v_mul_f32 v7, v7, v10\n"
    "v_mfma_f32_16x16x32_bf16 v[26:29], v[144:147], a[204:207], v[26:29]\n"
    "v_mul_f32 v48, v48, v10\n"
    "v_mul_f32 v49, v49, v10\n"
    "v_mfma_f32_16x16x32_bf16 v[26:29], v[148:151], a[208:211], v[26:29]\n"
    "v_mul_f32 v50, v50, v10\n"
    "v_mul_f32 v51, v51, v10\n"
    "v_mfma_f32_16x16x32_bf16 v[26:29], v[152:155], a[212:215], v[26:29]\n"
    "s_waitcnt lgkmcnt(0)\n"
    "v_cvt_pk_bf16_f32 v0, v12, v13\n"
    "v_cvt_pk_bf16_f32 v1, v14, v15\n"
    "v_mfma_f32_16x16x32_bf16 v[12:15], a[0:3], v[68:71], 0\n"
    "ds_write_b64 v211, v[0:1] offset:4096\n"
    "v_mfma_f32_16x16x32_bf16 v[12:15], a[4:7], v[72:75], v[12:15]\n"
    "v_cvt_pk_bf16_f32 v0, v4, v5\n"
    "v_cvt_pk_bf16_f32 v1, v6, v7\n"
    "v_mfma_f32_16x16x32_bf16 v[12:15], a[8:11], v[76:79], v[12:15]\n"
    "v_cvt_pk_bf16_f32 v2, v48, v49\n"
    "v_cvt_pk_bf16_f32 v3, v50, v51\n"
    "v_mfma_f32_16x16x32_bf16 v[12:15], a[12:15], v[140:143], v[12:15]\n"
    "ds_write_b64 v212, v[0:1] offset:4096\n"
    "v_mfma_f32_16x16x32_bf16 v[12:15], a[16:19], v[18:21], v[12:15]\n"
    "v_cvt_pk_bf16_f32 v130, v26, v27\n"
    "v_cvt_pk_bf16_f32 v131, v28, v29\n"
    "v_mfma_f32_16x16x32_bf16 v[12:15], a[20:23], v[160:163], v[12:15]\n"
    "v_fma_f32 v34, v34, s68, v11\n"
    "v_fma_f32 v35, v35, s68, v11\n"
    "v_mfma_f32_16x16x32_bf16 v[4:7], a[24:27], v[68:71], 0\n"
    "ds_write_b64 v213, v[2:3] offset:4096\n"
    "v_mfma_f32_16x16x32_bf16 v[4:7], a[28:31], v[72:75], v[4:7]\n"
    "v_fma_f32 v82, v82, s68, v11\n"
    "v_fma_f32 v83, v83, s68, v11\n"
    "v_mfma_f32_16x16x32_bf16 v[4:7], a[32:35], v[76:79], v[4:7]\n"
    "v_fma_f32 v84, v84, s68, v11\n"
    "v_fma_f32 v85, v85, s68, v11\n"
    "v_mfma_f32_16x16x32_bf16 v[4:7], a[36:39], v[140:143], v[4:7]\n"
    "v_exp_f32 v32, v32\n"
    "v_mfma_f32_16x16x32_bf16 v[4:7], a[40:43], v[18:21], v[4:7]\n"
    "v_exp_f32 v33, v33\n"
    "v_mfma_f32_16x16x32_bf16 v[4:7], a[44:47], v[160:163], v[4:7]\n"
    "v_exp_f32 v34, v34\n"
    "v_mfma_f32_16x16x32_bf16 v[0:3], a[48:51], v[68:71], 0\n"
    "v_exp_f32 v35, v35\n"
    "v_mfma_f32_16x16x32_bf16 v[0:3], a[52:55], v[72:75], v[0:3]\n"
    "v_exp_f32 v82, v82\n"
    "v_mfma_f32_16x16x32_bf16 v[0:3], a[56:59], v[76:79], v[0:3]\n"
    "v_exp_f32 v83, v83\n"
    "v_mfma_f32_16x16x32_bf16 v[0:3], a[60:63], v[140:143], v[0:3]\n"
    "ds_read_b128 v[22:25], v205 offset:6144\n"
    "v_fma_f32 v132, v132, s68, v11\n"
    "v_mfma_f32_16x16x32_bf16 v[0:3], a[64:67], v[18:21], v[0:3]\n"
    "ds_read_b128 v[26:29], v206 offset:6144\n"
    "v_fma_f32 v133, v133, s68, v11\n"
    "v_mfma_f32_16x16x32_bf16 v[0:3], a[68:71], v[160:163], v[0:3]\n"
    "ds_read_b128 v[152:155], v205 offset:14336\n"
    "v_fma_f32 v134, v134, s68, v11\n"
    "v_mfma_f32_16x16x32_bf16 v[48:51], v[68:71], a[144:147], 0\n"
    "ds_read_b128 v[156:159], v206 offset:14336\n"
    "v_fma_f32 v135, v135, s68, v11\n"
    "v_mfma_f32_16x16x32_bf16 v[48:51], v[72:75], a[148:151], v[48:51]\n"
    "ds_read_b128 v[164:167], v205 offset:22528\n"
    "v_fma_f32 v168, v168, s68, v11\n"
    "v_mfma_f32_16x16x32_bf16 v[48:51], v[76:79], a[152:155], v[48:51]\n"
    "ds_read_b128 v[182:185], v206 offset:22528\n"
    "v_fma_f32 v169, v169, s68, v11\n"
    "v_mfma_f32_16x16x32_bf16 v[48:51], v[140:143], a[156:159], v[48:51]\n"
    "v_exp_f32 v84, v84\n"
    "v_mfma_f32_16x16x32_bf16 v[48:51], v[18:21], a[160:163], v[48:51]\n"
    "v_exp_f32 v85, v85\n"
    "v_mfma_f32_16x16x32_bf16 v[48:51], v[160:163], a[164:167], v[48:51]\n"
    "v_fma_f32 v170, v170, s68, v11\n"
    "v_fma_f32 v171, v171, s68, v11\n"
    "v_mfma_f32_16x16x32_bf16 v[124:127], v[68:71], a[168:171], 0\n"
    "v_cvt_pk_bf16_f32 v30, v32, v33\n"
    "v_cvt_pk_bf16_f32 v31, v34, v35\n"
    "v_mfma_f32_16x16x32_bf16 v[124:127], v[72:75], a[172:175], v[124:127]\n"
    "v_cvt_pk_bf16_f32 v32, v82, v83\n"
    "v_cvt_pk_bf16_f32 v33, v84, v85\n"
    "v_mfma_f32_16x16x32_bf16 v[124:127], v[76:79], a[176:179], v[124:127]\n"
    "v_cvt_pk_bf16_f32 v44, v12, v13\n"
    "v_cvt_pk_bf16_f32 v45, v14, v15\n"
    "v_mfma_f32_16x16x32_bf16 v[10:13], v[220:223], v[30:33], 0\n"
    "v_exp_f32 v132, v132\n"
    "v_mfma_f32_16x16x32_bf16 v[124:127], v[140:143], a[180:183], v[124:127]\n"
    "v_exp_f32 v133, v133\n"
    "v_mfma_f32_16x16x32_bf16 v[124:127], v[18:21], a[184:187], v[124:127]\n"
    "v_exp_f32 v134, v134\n"
    "v_mfma_f32_16x16x32_bf16 v[124:127], v[160:163], a[188:191], v[124:127]\n"
    "v_exp_f32 v135, v135\n"
    "v_mfma_f32_16x16x32_bf16 v[82:85], v[68:71], a[192:195], 0\n"
    "v_exp_f32 v168, v168\n"
    "v_mfma_f32_16x16x32_bf16 v[82:85], v[72:75], a[196:199], v[82:85]\n"
    "v_exp_f32 v169, v169\n"
    "v_mfma_f32_16x16x32_bf16 v[82:85], v[76:79], a[200:203], v[82:85]\n"
    "v_exp_f32 v170, v170\n"
    "v_mfma_f32_16x16x32_bf16 v[82:85], v[140:143], a[204:207], v[82:85]\n"
    "v_exp_f32 v171, v171\n"
    "v_mfma_f32_16x16x32_bf16 v[82:85], v[18:21], a[208:211], v[82:85]\n"
    "v_cvt_pk_bf16_f32 v18, v132, v133\n"
    "v_cvt_pk_bf16_f32 v19, v134, v135\n"
    "v_mfma_f32_16x16x32_bf16 v[82:85], v[160:163], a[212:215], v[82:85]\n"
    "s_waitcnt lgkmcnt(0)\n"
    "v_cvt_pk_bf16_f32 v20, v168, v169\n"
    "v_cvt_pk_bf16_f32 v21, v170, v171\n"
    "v_mfma_f32_16x16x32_bf16 v[68:71], a[0:3], v[22:25], 0\n"
    "v_cvt_pk_bf16_f32 v46, v4, v5\n"
    "v_cvt_pk_bf16_f32 v47, v6, v7\n"
    "v_mfma_f32_16x16x32_bf16 v[10:13], v[220:223], v[18:21], v[10:13]\n"
    "v_cvt_pk_bf16_f32 v58, v0, v1\n"
    "v_cvt_pk_bf16_f32 v59, v2, v3\n"
    "v_mfma_f32_16x16x32_bf16 v[0:3], v[88:91], v[30:33], 0\n"
    "v_cvt_pk_bf16_f32 v116, v48, v49\n"
    "v_cvt_pk_bf16_f32 v117, v50, v51\n"
    "v_mfma_f32_16x16x32_bf16 v[0:3], v[92:95], v[18:21], v[0:3]\n"
    "v_cvt_pk_bf16_f32 v124, v124, v125\n"
    "v_cvt_pk_bf16_f32 v125, v126, v127\n"
    "v_mfma_f32_16x16x32_bf16 v[4:7], v[96:99], v[30:33], 0\n"
    "v_rcp_f32 v10, v10\n"
    "v_mfma_f32_16x16x32_bf16 v[4:7], v[100:103], v[18:21], v[4:7]\n"
    "v_cvt_pk_bf16_f32 v132, v82, v83\n"
    "v_cvt_pk_bf16_f32 v133, v84, v85\n"
    "v_mfma_f32_16x16x32_bf16 v[12:15], v[104:107], v[30:33], 0\n"
    "v_mul_f32 v0, v0, v10\n"
    "v_mul_f32 v1, v1, v10\n"
    "v_mfma_f32_16x16x32_bf16 v[12:15], v[108:111], v[18:21], v[12:15]\n"
    "v_mul_f32 v2, v2, v10\n"
    "v_mul_f32 v3, v3, v10\n"
    "v_mfma_f32_16x16x32_bf16 v[68:71], a[4:7], v[26:29], v[68:71]\n"
    "v_mul_f32 v4, v4, v10\n"
    "v_mul_f32 v5, v5, v10\n"
    "v_mfma_f32_16x16x32_bf16 v[68:71], a[8:11], v[152:155], v[68:71]\n"
    "v_mul_f32 v6, v6, v10\n"
    "v_mul_f32 v7, v7, v10\n"
    "v_mfma_f32_16x16x32_bf16 v[68:71], a[12:15], v[156:159], v[68:71]\n"
    "v_mul_f32 v12, v12, v10\n"
    "v_mul_f32 v13, v13, v10\n"
    "v_mfma_f32_16x16x32_bf16 v[68:71], a[16:19], v[164:167], v[68:71]\n"
    "v_mul_f32 v14, v14, v10\n"
    "v_mul_f32 v15, v15, v10\n"
    "v_mfma_f32_16x16x32_bf16 v[68:71], a[20:23], v[182:185], v[68:71]\n"
    "v_cvt_pk_bf16_f32 v0, v0, v1\n"
    "v_cvt_pk_bf16_f32 v1, v2, v3\n"
    "v_mfma_f32_16x16x32_bf16 v[18:21], a[24:27], v[22:25], 0\n"
    "ds_write_b64 v211, v[0:1] offset:6144\n"
    "v_mfma_f32_16x16x32_bf16 v[18:21], a[28:31], v[26:29], v[18:21]\n"
    "v_cvt_pk_bf16_f32 v0, v4, v5\n"
    "v_cvt_pk_bf16_f32 v1, v6, v7\n"
    "v_mfma_f32_16x16x32_bf16 v[18:21], a[32:35], v[152:155], v[18:21]\n"
    "v_cvt_pk_bf16_f32 v2, v12, v13\n"
    "v_cvt_pk_bf16_f32 v3, v14, v15\n"
    "v_mfma_f32_16x16x32_bf16 v[18:21], a[36:39], v[156:159], v[18:21]\n"
    "ds_write_b64 v212, v[0:1] offset:6144\n"
    "v_mfma_f32_16x16x32_bf16 v[18:21], a[40:43], v[164:167], v[18:21]\n"
    "v_cvt_pk_bf16_f32 v48, v68, v69\n"
    "v_cvt_pk_bf16_f32 v49, v70, v71\n"
    "v_mfma_f32_16x16x32_bf16 v[18:21], a[44:47], v[182:185], v[18:21]\n"
    "v_mov_b64 v[168:169], v[80:81]\n"
    "v_mov_b64 v[174:175], v[80:81]\n"
    "v_mfma_f32_16x16x32_bf16 v[4:7], a[48:51], v[22:25], 0\n"
    "ds_write_b64 v213, v[2:3] offset:6144\n"
    "v_mfma_f32_16x16x32_bf16 v[4:7], a[52:55], v[26:29], v[4:7]\n"
    "v_mov_b64 v[180:181], v[80:81]\n"
    "v_mfma_f32_16x16x32_bf16 v[4:7], a[56:59], v[152:155], v[4:7]\n"
    "v_mfma_f32_16x16x32_bf16 v[4:7], a[60:63], v[156:159], v[4:7]\n"
    "v_cvt_pk_bf16_f32 v50, v18, v19\n"
    "v_cvt_pk_bf16_f32 v51, v20, v21\n"
    "v_mfma_f32_16x16x32_bf16 v[4:7], a[64:67], v[164:167], v[4:7]\n"
    "v_mfma_f32_16x16x32_bf16 v[4:7], a[68:71], v[182:185], v[4:7]\n"
    "s_waitcnt lgkmcnt(0)\n"
    "s_barrier\n"
    "v_mfma_f32_16x16x32_bf16 v[0:3], v[22:25], a[144:147], 0\n"
    "ds_read_b128 v[136:139], v16 offset:0\n"
    "v_mfma_f32_16x16x32_bf16 v[0:3], v[26:29], a[148:151], v[0:3]\n"
    "ds_read_b128 v[140:143], v17 offset:1024\n"
    "v_mfma_f32_16x16x32_bf16 v[0:3], v[152:155], a[152:155], v[0:3]\n"
    "ds_read_b128 v[144:147], v16 offset:2048\n"
    "v_mfma_f32_16x16x32_bf16 v[0:3], v[156:159], a[156:159], v[0:3]\n"
    "v_cvt_pk_bf16_f32 v62, v4, v5\n"
    "v_cvt_pk_bf16_f32 v63, v6, v7\n"
    "v_mfma_f32_16x16x32_bf16 v[0:3], v[164:167], a[160:163], v[0:3]\n"
    "ds_read_b128 v[148:151], v17 offset:3072\n"
    "v_mfma_f32_16x16x32_bf16 v[0:3], v[182:185], a[164:167], v[0:3]\n"
    "ds_read_b128 v[170:173], v8 offset:24576\n"
    "v_mfma_f32_16x16x32_bf16 v[4:7], v[22:25], a[168:171], 0\n"
    "ds_read_b128 v[176:179], v9 offset:24576\n"
    "v_mfma_f32_16x16x32_bf16 v[4:7], v[26:29], a[172:175], v[4:7]\n"
    "v_mfma_f32_16x16x32_bf16 v[4:7], v[152:155], a[176:179], v[4:7]\n"
    "v_mfma_f32_16x16x32_bf16 v[4:7], v[156:159], a[180:183], v[4:7]\n"
    "v_mfma_f32_16x16x32_bf16 v[4:7], v[164:167], a[184:187], v[4:7]\n"
    "v_mfma_f32_16x16x32_bf16 v[4:7], v[182:185], a[188:191], v[4:7]\n"
    "v_cvt_pk_bf16_f32 v118, v0, v1\n"
    "v_cvt_pk_bf16_f32 v119, v2, v3\n"
    "v_mfma_f32_16x16x32_bf16 v[196:199], v[22:25], a[192:195], 0\n"
    "v_mfma_f32_16x16x32_bf16 v[196:199], v[26:29], a[196:199], v[196:199]\n"
    "v_mfma_f32_16x16x32_bf16 v[196:199], v[152:155], a[200:203], v[196:199]\n"
    "v_mfma_f32_16x16x32_bf16 v[196:199], v[156:159], a[204:207], v[196:199]\n"
    "v_mfma_f32_16x16x32_bf16 v[196:199], v[164:167], a[208:211], v[196:199]\n"
    "v_mfma_f32_16x16x32_bf16 v[196:199], v[182:185], a[212:215], v[196:199]\n"
    "v_cvt_pk_bf16_f32 v126, v4, v5\n"
    "v_cvt_pk_bf16_f32 v127, v6, v7\n"
    "s_nop 1\n"
    "s_waitcnt lgkmcnt(0)\n"
    "v_mfma_f32_16x16x32_bf16 v[136:139], v[36:39], v[64:67], v[136:139]\n"
    "ds_read_b128 v[0:3], v207 offset:0\n"
    "v_mfma_f32_16x16x32_bf16 v[136:139], v[52:55], v[168:171], v[136:139]\n"
    "ds_read_b128 v[4:7], v208 offset:0\n"
    "v_mfma_f32_16x16x32_bf16 v[140:143], v[40:43], v[64:67], v[140:143]\n"
    "ds_read_b128 v[8:11], v207 offset:8192\n"
    "v_mfma_f32_16x16x32_bf16 v[140:143], v[54:57], v[172:175], v[140:143]\n"
    "ds_read_b128 v[12:15], v208 offset:8192\n"
    "v_cvt_pk_bf16_f32 v134, v196, v197\n"
    "v_mfma_f32_16x16x32_bf16 v[144:147], v[44:47], v[64:67], v[144:147]\n"
    "ds_read_b128 v[16:19], v207 offset:16384\n"
    "v_max3_f32 v20, v136, v137, v138\n"
    "v_mfma_f32_16x16x32_bf16 v[144:147], v[58:61], v[174:177], v[144:147]\n"
    "ds_read_b128 v[22:25], v208 offset:16384\n"
    "v_cvt_pk_bf16_f32 v135, v198, v199\n"
    "v_mfma_f32_16x16x32_bf16 v[148:151], v[48:51], v[64:67], v[148:151]\n"
    "v_max3_f32 v21, v139, v140, v141\n"
    "v_max3_f32 v20, v20, v142, v143\n"
    "v_mfma_f32_16x16x32_bf16 v[148:151], v[60:63], v[178:181], v[148:151]\n"
    "ds_read_b128 v[26:29], v207 offset:2048\n"
    "s_waitcnt lgkmcnt(6)\n"
    "v_mfma_f32_16x16x32_bf16 v[30:33], a[216:219], v[0:3], 0\n"
    "s_waitcnt lgkmcnt(5)\n"
    "v_max3_f32 v21, v21, v144, v145\n"
    "v_max3_f32 v20, v20, v146, v147\n"
    "v_mfma_f32_16x16x32_bf16 v[30:33], a[220:223], v[4:7], v[30:33]\n"
    "ds_read_b128 v[34:37], v208 offset:2048\n"
    "s_waitcnt lgkmcnt(5)\n"
    "v_mfma_f32_16x16x32_bf16 v[30:33], a[224:227], v[8:11], v[30:33]\n"
    "ds_read_b128 v[38:41], v207 offset:10240\n"
    "v_max3_f32 v21, v21, v148, v149\n"
    "s_waitcnt lgkmcnt(5)\n"
    "v_mfma_f32_16x16x32_bf16 v[30:33], a[228:231], v[12:15], v[30:33]\n"
    "s_waitcnt lgkmcnt(4)\n"
    "v_max3_f32 v20, v20, v150, v151\n"
    "v_mfma_f32_16x16x32_bf16 v[30:33], a[232:235], v[16:19], v[30:33]\n"
    "s_waitcnt lgkmcnt(3)\n"
    "v_max_f32 v20, v20, v21\n"
    "v_mfma_f32_16x16x32_bf16 v[30:33], a[236:239], v[22:25], v[30:33]\n"
    "v_mov_b32 v21, v20\n"
    "v_mfma_f32_16x16x32_bf16 v[42:45], a[240:243], v[0:3], 0\n"
    "ds_read_b128 v[46:49], v208 offset:10240\n"
    "v_mfma_f32_16x16x32_bf16 v[42:45], a[244:247], v[4:7], v[42:45]\n"
    "v_permlane32_swap_b32 v20, v21\n"
    "v_mfma_f32_16x16x32_bf16 v[42:45], a[248:251], v[8:11], v[42:45]\n"
    "v_max_f32 v20, v20, v21\n"
    "v_mfma_f32_16x16x32_bf16 v[42:45], a[252:255], v[12:15], v[42:45]\n"
    "ds_read_b128 v[50:53], v207 offset:18432\n"
    "v_mov_b32 v21, v20\n"
    "v_mfma_f32_16x16x32_bf16 v[42:45], v[224:227], v[16:19], v[42:45]\n"
    "ds_read_b128 v[54:57], v208 offset:18432\n"
    "v_cvt_pk_bf16_f32 v30, v30, v31\n"
    "v_mfma_f32_16x16x32_bf16 v[42:45], v[228:231], v[22:25], v[42:45]\n"
    "v_permlane16_swap_b32 v20, v21\n"
    "v_mfma_f32_16x16x32_bf16 v[58:61], v[232:235], v[0:3], 0\n"
    "v_max_f32 v20, v20, v21\n"
    "v_cvt_pk_bf16_f32 v31, v32, v33\n"
    "v_mfma_f32_16x16x32_bf16 v[58:61], v[236:239], v[4:7], v[58:61]\n"
    "v_mul_f32 v20, s71, v20\n"
    "v_mfma_f32_16x16x32_bf16 v[58:61], v[240:243], v[8:11], v[58:61]\n"
    "v_fma_f32 v136, v136, s68, v20\n"
    "v_cvt_pk_bf16_f32 v32, v42, v43\n"
    "v_mfma_f32_16x16x32_bf16 v[58:61], v[244:247], v[12:15], v[58:61]\n"
    "v_cvt_pk_bf16_f32 v33, v44, v45\n"
    "v_fma_f32 v137, v137, s68, v20\n"
    "v_mfma_f32_16x16x32_bf16 v[58:61], v[248:251], v[16:19], v[58:61]\n"
    "buffer_store_dwordx4 v[30:33], v214, s[44:47], s84 offen offset:0\n"
    "v_mfma_f32_16x16x32_bf16 v[58:61], v[252:255], v[22:25], v[58:61]\n"
    "ds_read_b128 v[0:3], v207 offset:4096\n"
    "v_fma_f32 v138, v138, s68, v20\n"
    "s_waitcnt lgkmcnt(4)\n"
    "v_mfma_f32_16x16x32_bf16 v[4:7], a[216:219], v[26:29], 0\n"
    "ds_read_b128 v[8:11], v208 offset:4096\n"
    "v_fma_f32 v139, v139, s68, v20\n"
    "v_mfma_f32_16x16x32_bf16 v[4:7], a[220:223], v[34:37], v[4:7]\n"
    "ds_read_b128 v[12:15], v207 offset:12288\n"
    "v_fma_f32 v140, v140, s68, v20\n"
    "v_mfma_f32_16x16x32_bf16 v[4:7], a[224:227], v[38:41], v[4:7]\n"
    "ds_read_b128 v[16:19], v208 offset:12288\n"
    "v_cvt_pk_bf16_f32 v22, v58, v59\n"
    "s_waitcnt lgkmcnt(6)\n"
    "v_mfma_f32_16x16x32_bf16 v[4:7], a[228:231], v[46:49], v[4:7]\n"
    "ds_read_b128 v[30:33], v207 offset:20480\n"
    "v_cvt_pk_bf16_f32 v23, v60, v61\n"
    "s_waitcnt lgkmcnt(6)\n"
    "v_mfma_f32_16x16x32_bf16 v[4:7], a[232:235], v[50:53], v[4:7]\n"
    "buffer_store_dwordx2 v[22:23], v215, s[44:47], s84 offen offset:0\n"
    "s_waitcnt lgkmcnt(5)\n"
    "v_mfma_f32_16x16x32_bf16 v[4:7], a[236:239], v[54:57], v[4:7]\n"
    "ds_read_b128 v[42:45], v208 offset:20480\n"
    "v_fma_f32 v141, v141, s68, v20\n"
    "v_mfma_f32_16x16x32_bf16 v[22:25], a[240:243], v[26:29], 0\n"
    "v_fma_f32 v142, v142, s68, v20\n"
    "v_fma_f32 v143, v143, s68, v20\n"
    "v_mfma_f32_16x16x32_bf16 v[22:25], a[244:247], v[34:37], v[22:25]\n"
    "v_exp_f32 v136, v136\n"
    "v_mfma_f32_16x16x32_bf16 v[22:25], a[248:251], v[38:41], v[22:25]\n"
    "v_exp_f32 v137, v137\n"
    "v_mfma_f32_16x16x32_bf16 v[22:25], a[252:255], v[46:49], v[22:25]\n"
    "v_exp_f32 v138, v138\n"
    "v_mfma_f32_16x16x32_bf16 v[22:25], v[224:227], v[50:53], v[22:25]\n"
    "v_exp_f32 v139, v139\n"
    "v_mfma_f32_16x16x32_bf16 v[22:25], v[228:231], v[54:57], v[22:25]\n"
    "v_exp_f32 v140, v140\n"
    "v_mfma_f32_16x16x32_bf16 v[58:61], v[232:235], v[26:29], 0\n"
    "v_exp_f32 v141, v141\n"
    "v_mfma_f32_16x16x32_bf16 v[58:61], v[236:239], v[34:37], v[58:61]\n"
    "v_exp_f32 v142, v142\n"
    "v_mfma_f32_16x16x32_bf16 v[58:61], v[240:243], v[38:41], v[58:61]\n"
    "v_exp_f32 v143, v143\n"
    "v_mfma_f32_16x16x32_bf16 v[58:61], v[244:247], v[46:49], v[58:61]\n"
    "v_fma_f32 v144, v144, s68, v20\n"
    "v_fma_f32 v145, v145, s68, v20\n"
    "v_mfma_f32_16x16x32_bf16 v[58:61], v[248:251], v[50:53], v[58:61]\n"
    "v_fma_f32 v146, v146, s68, v20\n"
    "v_fma_f32 v147, v147, s68, v20\n"
    "v_mfma_f32_16x16x32_bf16 v[58:61], v[252:255], v[54:57], v[58:61]\n"
    "s_barrier\n"
    "s_waitcnt lgkmcnt(0)\n"
    "v_mfma_f32_16x16x32_bf16 v[26:29], a[216:219], v[0:3], 0\n"
    "ds_read_b128 v[34:37], v207 offset:6144\n"
    "v_fma_f32 v148, v148, s68, v20\n"
    "v_mfma_f32_16x16x32_bf16 v[26:29], a[220:223], v[8:11], v[26:29]\n"
    "ds_read_b128 v[38:41], v208 offset:6144\n"
    "v_fma_f32 v149, v149, s68, v20\n"
    "v_mfma_f32_16x16x32_bf16 v[26:29], a[224:227], v[12:15], v[26:29]\n"
    "ds_read_b128 v[46:49], v207 offset:14336\n"
    "v_fma_f32 v150, v150, s68, v20\n"
    "v_mfma_f32_16x16x32_bf16 v[26:29], a[228:231], v[16:19], v[26:29]\n"
    "ds_read_b128 v[50:53], v208 offset:14336\n"
    "v_fma_f32 v151, v151, s68, v20\n"
    "v_mfma_f32_16x16x32_bf16 v[26:29], a[232:235], v[30:33], v[26:29]\n"
    "ds_read_b128 v[54:57], v207 offset:22528\n"
    "v_cvt_pk_bf16_f32 v62, v136, v137\n"
    "v_mfma_f32_16x16x32_bf16 v[26:29], a[236:239], v[42:45], v[26:29]\n"
    "ds_read_b128 v[66:69], v208 offset:22528\n"
    "v_cvt_pk_bf16_f32 v63, v138, v139\n"
    "v_mfma_f32_16x16x32_bf16 v[70:73], a[240:243], v[0:3], 0\n"
    "v_cvt_pk_bf16_f32 v64, v140, v141\n"
    "v_cvt_pk_bf16_f32 v65, v142, v143\n"
    "v_mfma_f32_16x16x32_bf16 v[70:73], a[244:247], v[8:11], v[70:73]\n"
    "v_exp_f32 v144, v144\n"
    "v_mfma_f32_16x16x32_bf16 v[74:77], v[220:223], v[62:65], 0\n"
    "v_exp_f32 v145, v145\n"
    "v_mfma_f32_16x16x32_bf16 v[70:73], a[248:251], v[12:15], v[70:73]\n"
    "v_exp_f32 v146, v146\n"
    "v_mfma_f32_16x16x32_bf16 v[70:73], a[252:255], v[16:19], v[70:73]\n"
    "v_exp_f32 v147, v147\n"
    "v_mfma_f32_16x16x32_bf16 v[70:73], v[224:227], v[30:33], v[70:73]\n"
    "v_exp_f32 v148, v148\n"
    "v_mfma_f32_16x16x32_bf16 v[70:73], v[228:231], v[42:45], v[70:73]\n"
    "v_exp_f32 v149, v149\n"
    "v_mfma_f32_16x16x32_bf16 v[78:81], v[232:235], v[0:3], 0\n"
    "v_exp_f32 v150, v150\n"
    "v_mfma_f32_16x16x32_bf16 v[78:81], v[236:239], v[8:11], v[78:81]\n"
    "v_exp_f32 v151, v151\n"
    "v_mfma_f32_16x16x32_bf16 v[78:81], v[240:243], v[12:15], v[78:81]\n"
    "v_cvt_pk_bf16_f32 v0, v144, v145\n"
    "v_cvt_pk_bf16_f32 v1, v146, v147\n"
    "v_mfma_f32_16x16x32_bf16 v[78:81], v[244:247], v[16:19], v[78:81]\n"
    "v_cvt_pk_bf16_f32 v2, v148, v149\n"
    "v_cvt_pk_bf16_f32 v3, v150, v151\n"
    "v_mfma_f32_16x16x32_bf16 v[78:81], v[248:251], v[30:33], v[78:81]\n"
    "v_cvt_pk_bf16_f32 v4, v4, v5\n"
    "v_cvt_pk_bf16_f32 v5, v6, v7\n"
    "v_mfma_f32_16x16x32_bf16 v[74:77], v[220:223], v[0:3], v[74:77]\n"
    "v_cvt_pk_bf16_f32 v6, v22, v23\n"
    "v_cvt_pk_bf16_f32 v7, v24, v25\n"
    "v_mfma_f32_16x16x32_bf16 v[8:11], v[112:115], v[62:65], 0\n"
    "buffer_store_dwordx4 v[4:7], v214, s[44:47], s85 offen offset:0\n"
    "v_mfma_f32_16x16x32_bf16 v[8:11], v[116:119], v[0:3], v[8:11]\n"
    "v_cvt_pk_bf16_f32 v12, v58, v59\n"
    "v_cvt_pk_bf16_f32 v13, v60, v61\n"
    "v_mfma_f32_16x16x32_bf16 v[4:7], v[120:123], v[62:65], 0\n"
    "v_rcp_f32 v14, v74\n"
    "v_mfma_f32_16x16x32_bf16 v[4:7], v[124:127], v[0:3], v[4:7]\n"
    "buffer_store_dwordx2 v[12:13], v215, s[44:47], s85 offen offset:0\n"
    "v_mfma_f32_16x16x32_bf16 v[16:19], v[128:131], v[62:65], 0\n"
    "v_cvt_pk_bf16_f32 v20, v26, v27\n"
    "v_mul_f32 v8, v8, v14\n"
    "v_mfma_f32_16x16x32_bf16 v[16:19], v[132:135], v[0:3], v[16:19]\n"
    "v_mul_f32 v9, v9, v14\n"
    "v_mul_f32 v10, v10, v14\n"
    "v_mfma_f32_16x16x32_bf16 v[78:81], v[252:255], v[42:45], v[78:81]\n"
    "v_mul_f32 v11, v11, v14\n"
    "v_mul_f32 v4, v4, v14\n"
    "v_mul_f32 v5, v5, v14\n"
    "v_mul_f32 v6, v6, v14\n"
    "v_mul_f32 v7, v7, v14\n"
    "v_mul_f32 v16, v16, v14\n"
    "v_mul_f32 v17, v17, v14\n"
    "v_mul_f32 v18, v18, v14\n"
    "v_mul_f32 v19, v19, v14\n"
    "v_cvt_pk_bf16_f32 v0, v8, v9\n"
    "v_cvt_pk_bf16_f32 v1, v10, v11\n"
    "v_cvt_pk_bf16_f32 v2, v4, v5\n"
    "v_cvt_pk_bf16_f32 v3, v6, v7\n"
    "v_cvt_pk_bf16_f32 v4, v16, v17\n"
    "v_cvt_pk_bf16_f32 v5, v18, v19\n"
    "ds_write_b64 v211, v[0:1] offset:0\n"
    "ds_write_b64 v212, v[2:3] offset:0\n"
    "ds_write_b64 v213, v[4:5] offset:0\n"
    "v_cvt_pk_bf16_f32 v21, v28, v29\n"
    "v_cvt_pk_bf16_f32 v22, v70, v71\n"
    "v_cvt_pk_bf16_f32 v23, v72, v73\n"
    "v_cvt_pk_bf16_f32 v0, v78, v79\n"
    "v_cvt_pk_bf16_f32 v1, v80, v81\n"
    "buffer_store_dwordx4 v[20:23], v214, s[44:47], s86 offen offset:0\n"
    "s_waitcnt lgkmcnt(0)\n"
    "s_barrier\n"
    "v_mfma_f32_16x16x32_bf16 v[2:5], a[216:219], v[34:37], 0\n"
    "ds_read_b128 v[6:9], v207 offset:0\n"
    "v_mfma_f32_16x16x32_bf16 v[2:5], a[220:223], v[38:41], v[2:5]\n"
    "ds_read_b128 v[10:13], v208 offset:0\n"
    "v_mfma_f32_16x16x32_bf16 v[2:5], a[224:227], v[46:49], v[2:5]\n"
    "buffer_store_dwordx2 v[0:1], v215, s[44:47], s86 offen offset:0\n"
    "v_mfma_f32_16x16x32_bf16 v[2:5], a[228:231], v[50:53], v[2:5]\n"
    "ds_read_b128 v[14:17], v207 offset:8192\n"
    "v_mfma_f32_16x16x32_bf16 v[2:5], a[232:235], v[54:57], v[2:5]\n"
    "ds_read_b128 v[18:21], v208 offset:8192\n"
    "v_mfma_f32_16x16x32_bf16 v[2:5], a[236:239], v[66:69], v[2:5]\n"
    "ds_read_b128 v[22:25], v207 offset:16384\n"
    "v_mfma_f32_16x16x32_bf16 v[26:29], a[240:243], v[34:37], 0\n"
    "ds_read_b128 v[30:33], v208 offset:16384\n"
    "v_mfma_f32_16x16x32_bf16 v[26:29], a[244:247], v[38:41], v[26:29]\n"
    "v_mfma_f32_16x16x32_bf16 v[26:29], a[248:251], v[46:49], v[26:29]\n"
    "v_mfma_f32_16x16x32_bf16 v[26:29], a[252:255], v[50:53], v[26:29]\n"
    "v_mfma_f32_16x16x32_bf16 v[26:29], v[224:227], v[54:57], v[26:29]\n"
    "v_mfma_f32_16x16x32_bf16 v[26:29], v[228:231], v[66:69], v[26:29]\n"
    "v_cvt_pk_bf16_f32 v0, v2, v3\n"
    "v_cvt_pk_bf16_f32 v1, v4, v5\n"
    "v_mfma_f32_16x16x32_bf16 v[42:45], v[232:235], v[34:37], 0\n"
    "v_mfma_f32_16x16x32_bf16 v[42:45], v[236:239], v[38:41], v[42:45]\n"
    "v_mfma_f32_16x16x32_bf16 v[42:45], v[240:243], v[46:49], v[42:45]\n"
    "v_mfma_f32_16x16x32_bf16 v[42:45], v[244:247], v[50:53], v[42:45]\n"
    "v_mfma_f32_16x16x32_bf16 v[42:45], v[248:251], v[54:57], v[42:45]\n"
    "v_mfma_f32_16x16x32_bf16 v[42:45], v[252:255], v[66:69], v[42:45]\n"
    "s_waitcnt lgkmcnt(1)\n"
    "v_cvt_pk_bf16_f32 v2, v26, v27\n"
    "v_cvt_pk_bf16_f32 v3, v28, v29\n"
    "v_mfma_f32_16x16x32_bf16 v[26:29], a[216:219], v[6:9], 0\n"
    "buffer_store_dwordx4 v[0:3], v214, s[44:47], s87 offen offset:0\n"
    "v_mfma_f32_16x16x32_bf16 v[26:29], a[220:223], v[10:13], v[26:29]\n"
    "v_mfma_f32_16x16x32_bf16 v[26:29], a[224:227], v[14:17], v[26:29]\n"
    "v_mfma_f32_16x16x32_bf16 v[26:29], a[228:231], v[18:21], v[26:29]\n"
    "v_mfma_f32_16x16x32_bf16 v[26:29], a[232:235], v[22:25], v[26:29]\n"
    "s_waitcnt lgkmcnt(0)\n"
    "v_cvt_pk_bf16_f32 v0, v42, v43\n"
    "v_cvt_pk_bf16_f32 v1, v44, v45\n"
    "v_mfma_f32_16x16x32_bf16 v[26:29], a[236:239], v[30:33], v[26:29]\n"
    "buffer_store_dwordx2 v[0:1], v215, s[44:47], s87 offen offset:0\n"
    "v_mfma_f32_16x16x32_bf16 v[2:5], a[240:243], v[6:9], 0\n"
    "v_mfma_f32_16x16x32_bf16 v[2:5], a[244:247], v[10:13], v[2:5]\n"
    "v_mfma_f32_16x16x32_bf16 v[2:5], a[248:251], v[14:17], v[2:5]\n"
    "v_mfma_f32_16x16x32_bf16 v[2:5], a[252:255], v[18:21], v[2:5]\n"
    "v_mfma_f32_16x16x32_bf16 v[2:5], v[224:227], v[22:25], v[2:5]\n"
    "v_mfma_f32_16x16x32_bf16 v[2:5], v[228:231], v[30:33], v[2:5]\n"
    "v_mfma_f32_16x16x32_bf16 v[34:37], v[232:235], v[6:9], 0\n"
    "v_cvt_pk_bf16_f32 v0, v26, v27\n"
    "v_cvt_pk_bf16_f32 v1, v28, v29\n"
    "v_mfma_f32_16x16x32_bf16 v[34:37], v[236:239], v[10:13], v[34:37]\n"
    "v_mfma_f32_16x16x32_bf16 v[34:37], v[240:243], v[14:17], v[34:37]\n"
    "v_mfma_f32_16x16x32_bf16 v[34:37], v[244:247], v[18:21], v[34:37]\n"
    "v_mfma_f32_16x16x32_bf16 v[34:37], v[248:251], v[22:25], v[34:37]\n"
    "v_mfma_f32_16x16x32_bf16 v[34:37], v[252:255], v[30:33], v[34:37]\n"
    "v_cvt_pk_bf16_f32 v2, v2, v3\n"
    "v_cvt_pk_bf16_f32 v3, v4, v5\n"
    "buffer_store_dwordx4 v[0:3], v214, s[44:47], s65 offen offset:0\n"
    "s_nop 0\n"
    "s_nop 0\n"
    "s_nop 0\n"
    "s_nop 0\n"
    "s_nop 0\n"
    "v_cvt_pk_bf16_f32 v0, v34, v35\n"
    "v_cvt_pk_bf16_f32 v1, v36, v37\n"
    "buffer_store_dwordx2 v[0:1], v215, s[44:47], s65 offen offset:0\n"
    "s_branch LEND%=\n"
    :
    : [ka] "s"(__builtin_amdgcn_kernarg_segment_ptr()), [wv] "s"(wv), [bid] "s"(blockIdx.x), [code] "n"(32000)
    : "v0", "v1", "v2", "v3", "v4", "v5", "v6", "v7", "v8", "v9", "v10", "v11", "v12", "v13", "v14", "v15", "v16", "v17", "v18", "v19", "v20", "v21", "v22", "v23", "v24", "v25", "v26", "v27", "v28", "v29", "v30", "v31", "v32", "v33", "v34", "v35", "v36", "v37", "v38", "v39", "v40", "v41", "v42", "v43", "v44", "v45", "v46", "v47", "v48", "v49", "v50", "v51", "v52", "v53", "v54", "v55", "v56", "v57", "v58", "v59", "v60", "v61", "v62", "v63", "v64", "v65", "v66", "v67", "v68", "v69", "v70", "v71", "v72", "v73", "v74", "v75", "v76", "v77", "v78", "v79", "v80", "v81", "v82", "v83", "v84", "v85", "v86", "v87", "v88", "v89", "v90", "v91", "v92", "v93", "v94", "v95", "v96", "v97", "v98", "v99", "v100", "v101", "v102", "v103", "v104", "v105", "v106", "v107", "v108", "v109", "v110", "v111", "v112", "v113", "v114", "v115", "v116", "v117", "v118", "v119", "v120", "v121", "v122", "v123", "v124", "v125", "v126", "v127", "v128", "v129", "v130", "v131", "v132", "v133", "v134", "v135", "v136", "v137", "v138", "v139", "v140", "v141", "v142", "v143", "v144", "v145", "v146", "v147", "v148", "v149", "v150", "v151", "v152", "v153", "v154", "v155", "v156", "v157", "v158", "v159", "v160", "v161", "v162", "v163", "v164", "v165", "v166", "v167", "v168", "v169", "v170", "v171", "v172", "v173", "v174", "v175", "v176", "v177", "v178", "v179", "v180", "v181", "v182", "v183", "v184", "v185", "v186", "v187", "v188", "v189", "v190", "v191", "v192", "v193", "v194", "v195", "v196", "v197", "v198", "v199", "v200", "v201", "v202", "v203", "v204", "v205", "v206", "v207", "v208", "v209", "v210", "v211", "v212", "v213", "v214", "v215", "v216", "v217", "v218", "v219", "v220", "v221", "v222", "v223", "v224", "v225", "v226", "v227", "v228", "v229", "v230", "v231", "v232", "v233", "v234", "v235", "v236", "v237", "v238", "v239", "v240", "v241", "v242", "v243", "v244", "v245", "v246", "v247", "v248", "v249", "v250", "v251", "v252", "v253", "v254", "v255", "a0", "a1", "a2", "a3", "a4", "a5", "a6", "a7", "a8", "a9", "a10", "a11", "a12", "a13", "a14", "a15", "a16", "a17", "a18", "a19", "a20", "a21", "a22", "a23", "a24", "a25", "a26", "a27", "a28", "a29", "a30", "a31", "a32", "a33", "a34", "a35", "a36", "a37", "a38", "a39", "a40", "a41", "a42", "a43", "a44", "a45", "a46", "a47", "a48", "a49", "a50", "a51", "a52", "a53", "a54", "a55", "a56", "a57", "a58", "a59", "a60", "a61", "a62", "a63", "a64", "a65", "a66", "a67", "a68", "a69", "a70", "a71", "a72", "a73", "a74", "a75", "a76", "a77", "a78", "a79", "a80", "a81", "a82", "a83", "a84", "a85", "a86", "a87", "a88", "a89", "a90", "a91", "a92", "a93", "a94", "a95", "a96", "a97", "a98", "a99", "a100", "a101", "a102", "a103", "a104", "a105", "a106", "a107", "a108", "a109", "a110", "a111", "a112", "a113", "a114", "a115", "a116", "a117", "a118", "a119", "a120", "a121", "a122", "a123", "a124", "a125", "a126", "a127", "a128", "a129", "a130", "a131", "a132", "a133", "a134", "a135", "a136", "a137", "a138", "a139", "a140", "a141", "a142", "a143", "a144", "a145", "a146", "a147", "a148", "a149", "a150", "a151", "a152", "a153", "a154", "a155", "a156", "a157", "a158", "a159", "a160", "a161", "a162", "a163", "a164", "a165", "a166", "a167", "a168", "a169", "a170", "a171", "a172", "a173", "a174", "a175", "a176", "a177", "a178", "a179", "a180", "a181", "a182", "a183", "a184", "a185", "a186", "a187", "a188", "a189", "a190", "a191", "a192", "a193", "a194", "a195", "a196", "a197", "a198", "a199", "a200", "a201", "a202", "a203", "a204", "a205", "a206", "a207", "a208", "a209", "a210", "a211", "a212", "a213", "a214", "a215", "a216", "a217", "a218", "a219", "a220", "a221", "a222", "a223", "a224", "a225", "a226", "a227", "a228", "a229", "a230", "a231", "a232", "a233", "a234", "a235", "a236", "a237", "a238", "a239", "a240", "a241", "a242", "a243", "a244", "a245", "a246", "a247", "a248", "a249", "a250", "a251", "a252", "a253", "a254", "a255", "s4", "s5", "s6", "s7", "s8", "s9", "s10", "s11", "s12", "s13", "s14", "s15", "s16", "s17", "s18", "s19", "s20", "s21", "s22", "s23", "s24", "s25", "s26", "s27", "s28", "s29", "s30", "s31", "s32", "s33", "s34", "s35", "s36", "s37", "s38", "s39", "s40", "s41", "s42", "s43", "s44", "s45", "s46", "s47", "s48", "s49", "s50", "s51", "s52", "s53", "s54", "s55", "s56", "s57", "s58", "s59", "s60", "s61", "s62", "s63", "s64", "s65", "s66", "s67", "s68", "s69", "s70", "s71", "s72", "s73", "s74", "s75", "s76", "s77", "s78", "s79", "s80", "s81", "s82", "s83", "s84", "s85", "s86", "s87", "s88", "s89", "s90", "s91", "s92", "s93", "s94", "s95", "s96", "s97", "s98", "s99", "s100", "s101", "vcc", "scc", "memory");
}

extern "C" void kernel_launch(void* const* d_in, const int* in_sizes, int n_in, void* d_out, int out_size, void* d_ws, size_t ws_size,
                              hipStream_t stream) {
  (void)hipFuncSetAttribute((const void*)swin_window_attn_kernel, hipFuncAttributeMaxDynamicSharedMemorySize, 163840);
  swin_window_attn_kernel<<<256, 256, 163840, stream>>>(d_in[0], d_in[1], d_in[2], d_in[3], d_in[4], d_in[5], d_out);
}
```
